```python
import torch

def _softmax(x, axis=-1):
    return torch.softmax(x, dim=axis)
def _gelu(x):
    return torch.nn.functional.gelu(x, approximate="tanh")
def _silu(x):
    return torch.nn.functional.silu(x)
def _sigmoid(x):
    return torch.sigmoid(x)
def _layernorm(x, axis=-1, eps=1e-5):
    m = x.mean(dim=axis, keepdim=True)
    v = x.var(dim=axis, keepdim=True, unbiased=False)
    return (x - m) / torch.sqrt(v + eps)
def _scatter_add(tgt, idx, src, axis=0):
    return tgt.clone().index_add_(axis, idx, src)
def _segment_sum(src, seg, n):
    out = torch.zeros((n,) + src.shape[1:], dtype=src.dtype)
    return out.index_add_(0, seg, src)
def _pad(x, pw):
    flat = tuple(v for pair in reversed(pw) for v in pair)
    return torch.nn.functional.pad(x, flat)

kernel_name = 'sequential_preLN__spectral_gfnet__geglu_ffn__prompt'

def setup_inputs():

    _w = (lambda a: (lambda t: t.to(torch.bfloat16) if t.is_floating_point() else t)(torch.as_tensor(a)))
    def _A_setup():
        B, S, D = 1, 2048, 512
        Df = D // 2 + 1
        return {
            "x": _w((lambda *s: torch.randn(*s, dtype=torch.bfloat16) * 0.05)(B, S, D)),
            "w_r": _w((lambda *s: torch.randn(*s, dtype=torch.bfloat16) * 0.4)(S, Df) * 0.02),
            "w_i": _w((lambda *s: torch.randn(*s, dtype=torch.bfloat16) * 0.4)(S, Df) * 0.02),
        }
    _A_in = _A_setup()

    def _B_setup():
        B, S, D, F = 1, 2048, 512, 1376
        return {
            "x": _w((lambda *s: torch.randn(*s, dtype=torch.bfloat16) * 0.05)(B, S, D)),
            "w_gate": _w((lambda *s: torch.randn(*s, dtype=torch.bfloat16) * 0.05)(D, F)),
            "w_up": _w((lambda *s: torch.randn(*s, dtype=torch.bfloat16) * 0.05)(D, F)),
            "w_down": _w((lambda *s: torch.randn(*s, dtype=torch.bfloat16) * 0.05)(F, D)),
        }
    _B_in = _B_setup()

    return {
        "A_x": _A_in["x"],
        "A_w_r": _A_in["w_r"],
        "A_w_i": _A_in["w_i"],
        "B_x": _B_in["x"],
        "B_w_gate": _B_in["w_gate"],
        "B_w_up": _B_in["w_up"],
        "B_w_down": _B_in["w_down"],
    }

def reference(A_x, A_w_r, A_w_i, B_x, B_w_gate, B_w_up, B_w_down):
    def _A_ref(x, w_r, w_i):
        B, S, D = x.shape
        X = (lambda x, axes=(-2,-1): torch.fft.rfft2(x.float(), dim=axes))(x, axes=(-2, -1))
        W = w_r + 1j * w_i
        X = X * W
        y = (lambda x, s, axes=(-2,-1): torch.fft.irfft2(x, s=s, dim=axes).to(torch.bfloat16))(X, (S, D), axes=(-2, -1))
        return y

    def _B_ref(x, w_gate, w_up, w_down):
        return (_gelu(x @ w_gate) * (x @ w_up)) @ w_down

    x = A_x
    h = x + _A_ref(_layernorm(x, axis=-1), A_w_r, A_w_i)
    h2 = h + _B_ref(_layernorm(h, axis=-1), B_w_gate, B_w_up, B_w_down)
    return h2
```

```cpp
#include <hip/hip_runtime.h>
#include <hip/hip_bf16.h>
#include <stdint.h>

typedef float f2 __attribute__((ext_vector_type(2)));
typedef float f4 __attribute__((ext_vector_type(4)));
typedef unsigned u4 __attribute__((ext_vector_type(4)));
typedef unsigned u2 __attribute__((ext_vector_type(2)));
typedef short bf8 __attribute__((ext_vector_type(8)));
typedef unsigned short us;

#define WAVE_BAR() __builtin_amdgcn_wave_barrier()

__device__ __forceinline__ float bf2f(unsigned h) { return __uint_as_float(h << 16); }
__device__ __forceinline__ float bf2f_hi(unsigned h) { return __uint_as_float(h & 0xffff0000u); }
__device__ __forceinline__ unsigned f2bf(float f) {
  __bf16 b = (__bf16)f;
  return (unsigned)__builtin_bit_cast(unsigned short, b);
}
__device__ __forceinline__ float rbf(float f) { return bf2f(f2bf(f)); }
__device__ __forceinline__ unsigned pack2(float lo, float hi) { return f2bf(lo) | (f2bf(hi) << 16); }

__device__ __forceinline__ f2 cmul(f2 a, f2 b) { return (f2){a.x * b.x - a.y * b.y, a.x * b.y + a.y * b.x}; }
__device__ __forceinline__ f2 cmulc(f2 a, f2 b) { return (f2){a.x * b.x + a.y * b.y, a.y * b.x - a.x * b.y}; }
template <bool INV> __device__ __forceinline__ f2 rot(f2 a) { return INV ? (f2){-a.y, a.x} : (f2){a.y, -a.x}; }

template <bool INV> __device__ __forceinline__ void dft8(f2 (&v)[8]) {
  const float r = 0.70710678118654752f;
  f2 a0 = v[0] + v[4], a1 = v[0] - v[4], a2 = v[2] + v[6], a3 = rot<INV>(v[2] - v[6]);
  f2 a4 = v[1] + v[5], a5 = v[1] - v[5], a6 = v[3] + v[7], a7 = rot<INV>(v[3] - v[7]);
  f2 e0 = a0 + a2, e2 = a0 - a2, e1 = a1 + a3, e3 = a1 - a3;
  f2 o0 = a4 + a6, o2 = a4 - a6, o1 = a5 + a7, o3 = a5 - a7;
  o1 = (o1 + rot<INV>(o1)) * r;
  o2 = rot<INV>(o2);
  o3 = (rot<INV>(o3) - o3) * r;
  v[0] = e0 + o0; v[4] = e0 - o0;
  v[1] = e1 + o1; v[5] = e1 - o1;
  v[2] = e2 + o2; v[6] = e2 - o2;
  v[3] = e3 + o3; v[7] = e3 - o3;
}
template <bool INV> __device__ __forceinline__ void dft4(f2& x0, f2& x1, f2& x2, f2& x3) {
  f2 a0 = x0 + x2, a1 = x0 - x2, a2 = x1 + x3, a3 = rot<INV>(x1 - x3);
  x0 = a0 + a2; x2 = a0 - a2; x1 = a1 + a3; x3 = a1 - a3;
}
__device__ __forceinline__ void make_tw(f2 (&t)[8], int num, float c2) {
  float s, c;
  sincospif(-(float)num * c2, &s, &c);
  t[1] = (f2){c, s};
  t[2] = cmul(t[1], t[1]);
  t[3] = cmul(t[2], t[1]);
  t[4] = cmul(t[2], t[2]);
  t[5] = cmul(t[4], t[1]);
  t[6] = cmul(t[3], t[3]);
  t[7] = cmul(t[4], t[3]);
}

template <int ctrl, int rm> __device__ __forceinline__ float dpp_add(float v) {
  int t = __builtin_amdgcn_update_dpp(0, __float_as_int(v), ctrl, rm, 0xf, false);
  return v + __int_as_float(t);
}
__device__ __forceinline__ float wave_sum(float v) {
  v = dpp_add<0x111, 0xf>(v);
  v = dpp_add<0x112, 0xf>(v);
  v = dpp_add<0x114, 0xf>(v);
  v = dpp_add<0x118, 0xf>(v);
  v = dpp_add<0x142, 0xa>(v);
  v = dpp_add<0x143, 0xc>(v);
  return __int_as_float(__builtin_amdgcn_readlane(__float_as_int(v), 63));
}

__device__ __forceinline__ void ln_pair(float (&a)[8], float (&b)[8]) {
  float sa = 0.f, sb = 0.f;
#pragma unroll
  for (int j = 0; j < 8; ++j) { sa += a[j]; sb += b[j]; }
  sa = wave_sum(sa); sb = wave_sum(sb);
  float ma = sa * (1.0f / 512.0f), mb = sb * (1.0f / 512.0f);
  float va = 0.f, vb = 0.f;
#pragma unroll
  for (int j = 0; j < 8; ++j) { float d = a[j] - ma; va += d * d; float e = b[j] - mb; vb += e * e; }
  va = wave_sum(va) * (1.0f / 512.0f); vb = wave_sum(vb) * (1.0f / 512.0f);
  float mra = rbf(ma), mrb = rbf(mb);
  float da = rbf(sqrtf(rbf(rbf(va) + 1e-5f))), db = rbf(sqrtf(rbf(rbf(vb) + 1e-5f)));
#pragma unroll
  for (int j = 0; j < 8; ++j) { a[j] = rbf(rbf(a[j] - mra) / da); b[j] = rbf(rbf(b[j] - mrb) / db); }
}

__device__ __forceinline__ int p_of_q(int q) {
  int T = q >> 3, e = q & 7;
  int k0 = T >> 5, k1 = (T >> 2) & 7, k2 = 2 * (T & 3) + (e >> 2), k3 = e & 3;
  return k0 + 8 * k1 + 64 * k2 + 512 * k3;
}

#define WSTR 584

__global__ __launch_bounds__(256) void k1_lnfft(const us* __restrict__ x, const us* __restrict__ wr,
                                                const us* __restrict__ wi, const us* __restrict__ wg,
                                                const us* __restrict__ wu, const us* __restrict__ wd,
                                                f2* __restrict__ X1, unsigned* __restrict__ Wt,
                                                f4* __restrict__ Wsp, us* __restrict__ W1t,
                                                us* __restrict__ W2t) {
  __shared__ __attribute__((aligned(16))) f2 lds[4 * WSTR];
  const int b = blockIdx.x, tid = threadIdx.x;
  const int w = tid >> 6, l = tid & 63;
  if (b < 256) {
    const int row = b * 8 + w * 2;
    u4 ra = *(const u4*)(x + (size_t)row * 512 + l * 8);
    u4 rb = *(const u4*)(x + (size_t)(row + 1) * 512 + l * 8);
    f2 t1[8], t2[8];
    make_tw(t1, l, 2.0f / 512.0f);
    make_tw(t2, l & 7, 2.0f / 64.0f);
    float a[8], c[8];
#pragma unroll
    for (int j = 0; j < 4; ++j) {
      a[2 * j] = bf2f(ra[j] & 0xffffu); a[2 * j + 1] = bf2f_hi(ra[j]);
      c[2 * j] = bf2f(rb[j] & 0xffffu); c[2 * j + 1] = bf2f_hi(rb[j]);
    }
    ln_pair(a, c);
    f2* L = lds + w * WSTR;
    unsigned* lp = (unsigned*)L;
    u4 p0, p1;
#pragma unroll
    for (int j = 0; j < 4; ++j) {
      p0[j] = (__float_as_uint(a[j]) >> 16) | (__float_as_uint(c[j]) & 0xffff0000u);
      p1[j] = (__float_as_uint(a[j + 4]) >> 16) | (__float_as_uint(c[j + 4]) & 0xffff0000u);
    }
    *(u4*)(lp + l * 8) = p0;
    *(u4*)(lp + l * 8 + 4) = p1;
    WAVE_BAR();
    f2 v[8];
#pragma unroll
    for (int m = 0; m < 8; ++m) { unsigned q = lp[l + 64 * m]; v[m] = (f2){bf2f(q & 0xffffu), bf2f_hi(q)}; }
    WAVE_BAR();
    dft8<false>(v);
#pragma unroll
    for (int k = 1; k < 8; ++k) v[k] = cmul(v[k], t1[k]);
#pragma unroll
    for (int k = 0; k < 8; ++k) L[k * 72 + l] = v[k];
    WAVE_BAR();
    const int hi = l >> 3, lo = l & 7;
#pragma unroll
    for (int n = 0; n < 8; ++n) v[n] = L[hi * 72 + n * 8 + lo];
    WAVE_BAR();
    dft8<false>(v);
#pragma unroll
    for (int k = 1; k < 8; ++k) v[k] = cmul(v[k], t2[k]);
#pragma unroll
    for (int k = 0; k < 8; ++k) L[hi * 72 + k * 9 + lo] = v[k];
    WAVE_BAR();
#pragma unroll
    for (int n = 0; n < 8; ++n) v[n] = L[hi * 72 + lo * 9 + n];
    WAVE_BAR();
    dft8<false>(v);
#pragma unroll
    for (int k = 0; k < 8; ++k) L[hi + 9 * lo + 72 * k] = v[k];
    __syncthreads();
    const int p = tid & 3;
    const f2* Lp = lds + p * WSTR;
#pragma unroll
    for (int i = 0; i < 4; ++i) {
      int k = (tid >> 2) + 64 * i;
      int kk = (512 - k) & 511;
      f2 z = Lp[k + (k >> 3)];
      f2 y = Lp[kk + (kk >> 3)];
      f4 o;
      if (k == 0) {
        f2 z256 = Lp[256 + 32];
        o = (f4){z.x, z256.x, z.y, z256.y};
      } else {
        o = (f4){0.5f * (z.x + y.x), 0.5f * (z.y - y.y), 0.5f * (z.y + y.y), 0.5f * (y.x - z.x)};
      }
      *(f4*)(X1 + (size_t)k * 2048 + b * 8 + 2 * p) = o;
    }
    return;
  }
  if (b < 384) {
    unsigned* tile = (unsigned*)lds;
    const int qi = (b - 256) >> 2, ci = (b - 256) & 3;
    const int c = tid & 63;
#pragma unroll
    for (int i = 0; i < 16; ++i) {
      int qq = (tid >> 6) + 4 * i;
      int pp = p_of_q(64 * qi + qq);
      unsigned r_ = wr[pp * 257 + 64 * ci + c], i_ = wi[pp * 257 + 64 * ci + c];
      tile[c * 65 + qq] = r_ | (i_ << 16);
    }
    __syncthreads();
#pragma unroll
    for (int i = 0; i < 16; ++i) {
      int cc = (tid >> 6) + 4 * i;
      Wt[(size_t)(64 * ci + cc) * 2048 + 64 * qi + c] = tile[cc * 65 + c];
    }
    return;
  }
  if (b < 392) {
    int q = (b - 384) * 256 + tid;
    int pp = p_of_q(q), pm = (2048 - pp) & 2047;
    float r0p = bf2f(wr[pp * 257]), i0p = bf2f(wi[pp * 257]);
    float r0m = bf2f(wr[pm * 257]), i0m = bf2f(wi[pm * 257]);
    float r2p = bf2f(wr[pp * 257 + 256]), i2p = bf2f(wi[pp * 257 + 256]);
    float r2m = bf2f(wr[pm * 257 + 256]), i2m = bf2f(wi[pm * 257 + 256]);
    const float sc = 0.25f / 1048576.0f;
    float s0r = r0p + r0m, s0i = i0p - i0m, s2r = r2p + r2m, s2i = i2p - i2m;
    Wsp[q] = (f4){(s0r + s2r) * sc, (s0i + s2i) * sc, (s0r - s2r) * sc, (s0i - s2i) * sc};
    return;
  }
  {
    int f0 = (b - 392) * 16 + w * 4;
#pragma unroll
    for (int i = 0; i < 4; ++i) {
      int fr = f0 + i;
      us vals[8];
      if (fr < 3072) {
        int nb = fr % 12, kb = (fr / 12) & 15, ft = fr / 192;
        const us* src = nb < 6 ? wg : wu;
        int lc = 16 * (nb % 6) + (l & 15);
        int col = 86 * ft + lc;
        int k0 = 32 * kb + 8 * (l >> 4);
#pragma unroll
        for (int j = 0; j < 8; ++j) vals[j] = lc < 86 ? src[(size_t)(k0 + j) * 1376 + col] : (us)0;
        u4 o;
#pragma unroll
        for (int j = 0; j < 4; ++j) o[j] = vals[2 * j] | ((unsigned)vals[2 * j + 1] << 16);
        *(u4*)(W1t + ((size_t)fr * 64 + l) * 8) = o;
      } else {
        int g = fr - 3072;
        int nb = g & 3, kb = (g >> 2) % 48, nt = g / 192;
        int col = 64 * nt + 16 * nb + (l & 15);
        int kp = 32 * kb + 8 * (l >> 4);
        int ft = kp / 96, c0 = kp % 96;
#pragma unroll
        for (int j = 0; j < 8; ++j) vals[j] = (c0 + j) < 86 ? wd[(size_t)(86 * ft + c0 + j) * 512 + col] : (us)0;
        u4 o;
#pragma unroll
        for (int j = 0; j < 4; ++j) o[j] = vals[2 * j] | ((unsigned)vals[2 * j + 1] << 16);
        *(u4*)(W2t + ((size_t)g * 64 + l) * 8) = o;
      }
    }
  }
}

__global__ __launch_bounds__(256) void k2_col(f2* __restrict__ X1, const unsigned* __restrict__ Wt,
                                              const f4* __restrict__ Wsp) {
  __shared__ __attribute__((aligned(16))) f2 lds[8 * 288];
  __shared__ __attribute__((aligned(16))) f2 nat[2048];
  const int c = blockIdx.x, t = threadIdx.x;
  f2* col = X1 + (size_t)c * 2048;
  f2 v[8];
#pragma unroll
  for (int m = 0; m < 8; ++m) v[m] = col[t + 256 * m];
  u4 w0 = *(const u4*)(Wt + (size_t)c * 2048 + t * 8);
  u4 w1 = *(const u4*)(Wt + (size_t)c * 2048 + t * 8 + 4);
  f2 t1[8], t2[8], t3[8];
  make_tw(t1, t, 2.0f / 2048.0f);
  make_tw(t2, t & 31, 2.0f / 256.0f);
  make_tw(t3, t & 3, 2.0f / 32.0f);
  const int g = t >> 5, lo5 = t & 31;
  f2* R = lds + g * 288;
  dft8<false>(v);
#pragma unroll
  for (int k = 1; k < 8; ++k) v[k] = cmul(v[k], t1[k]);
#pragma unroll
  for (int k = 0; k < 8; ++k) lds[k * 288 + t] = v[k];
  __syncthreads();
#pragma unroll
  for (int n = 0; n < 8; ++n) v[n] = R[n * 32 + lo5];
  WAVE_BAR();
  dft8<false>(v);
#pragma unroll
  for (int k = 1; k < 8; ++k) v[k] = cmul(v[k], t2[k]);
#pragma unroll
  for (int k = 0; k < 8; ++k) R[k * 36 + lo5] = v[k];
  WAVE_BAR();
  const int m3 = (t >> 2) & 7, q3 = t & 3;
#pragma unroll
  for (int n = 0; n < 8; ++n) v[n] = R[m3 * 36 + 4 * n + q3];
  WAVE_BAR();
  dft8<false>(v);
#pragma unroll
  for (int k = 1; k < 8; ++k) v[k] = cmul(v[k], t3[k]);
#pragma unroll
  for (int k = 0; k < 8; ++k) R[m3 * 34 + 4 * k + q3] = v[k];
  WAVE_BAR();
#pragma unroll
  for (int e = 0; e < 8; ++e) v[e] = R[m3 * 34 + 8 * q3 + e];
  WAVE_BAR();
  dft4<false>(v[0], v[1], v[2], v[3]);
  dft4<false>(v[4], v[5], v[6], v[7]);
  if (c != 0) {
    const float sc = 1.0f / 1048576.0f;
#pragma unroll
    for (int e = 0; e < 4; ++e) {
      f2 wa = (f2){bf2f(w0[e] & 0xffffu) * sc, bf2f_hi(w0[e]) * sc};
      f2 wb = (f2){bf2f(w1[e] & 0xffffu) * sc, bf2f_hi(w1[e]) * sc};
      v[e] = cmul(v[e], wa);
      v[e + 4] = cmul(v[e + 4], wb);
    }
  } else {
#pragma unroll
    for (int e = 0; e < 8; ++e) nat[p_of_q(t * 8 + e)] = v[e];
    __syncthreads();
#pragma unroll
    for (int e = 0; e < 8; ++e) {
      int pm = (2048 - p_of_q(t * 8 + e)) & 2047;
      f2 cm = nat[pm];
      cm.y = -cm.y;
      f4 ws = Wsp[t * 8 + e];
      v[e] = cmul(v[e], (f2){ws.x, ws.y}) + cmul(cm, (f2){ws.z, ws.w});
    }
  }
  dft4<true>(v[0], v[1], v[2], v[3]);
  dft4<true>(v[4], v[5], v[6], v[7]);
#pragma unroll
  for (int e = 0; e < 8; ++e) R[m3 * 34 + 8 * q3 + e] = v[e];
  WAVE_BAR();
#pragma unroll
  for (int k = 0; k < 8; ++k) v[k] = R[m3 * 34 + 4 * k + q3];
  WAVE_BAR();
#pragma unroll
  for (int k = 1; k < 8; ++k) v[k] = cmulc(v[k], t3[k]);
  dft8<true>(v);
#pragma unroll
  for (int n = 0; n < 8; ++n) R[m3 * 36 + 4 * n + q3] = v[n];
  WAVE_BAR();
#pragma unroll
  for (int k = 0; k < 8; ++k) v[k] = R[k * 36 + lo5];
  WAVE_BAR();
#pragma unroll
  for (int k = 1; k < 8; ++k) v[k] = cmulc(v[k], t2[k]);
  dft8<true>(v);
#pragma unroll
  for (int n = 0; n < 8; ++n) R[n * 32 + lo5] = v[n];
  __syncthreads();
#pragma unroll
  for (int k = 0; k < 8; ++k) v[k] = lds[k * 288 + t];
#pragma unroll
  for (int k = 1; k < 8; ++k) v[k] = cmulc(v[k], t1[k]);
  dft8<true>(v);
#pragma unroll
  for (int m = 0; m < 8; ++m) col[t + 256 * m] = v[m];
}

__global__ __launch_bounds__(256) void k3_ifft(const f2* __restrict__ X1, const us* __restrict__ x,
                                               us* __restrict__ h, us* __restrict__ ln2) {
  __shared__ __attribute__((aligned(16))) f2 lds[4 * WSTR];
  const int b = blockIdx.x, tid = threadIdx.x;
  const int w = tid >> 6, l = tid & 63;
  const int p = tid & 3;
  f4 o[4];
#pragma unroll
  for (int i = 0; i < 4; ++i) {
    int k = (tid >> 2) + 64 * i;
    o[i] = *(const f4*)(X1 + (size_t)k * 2048 + b * 8 + 2 * p);
  }
  const int row = b * 8 + w * 2;
  u4 ra = *(const u4*)(x + (size_t)row * 512 + l * 8);
  u4 rb = *(const u4*)(x + (size_t)(row + 1) * 512 + l * 8);
  f2 t1[8], t2[8];
  make_tw(t1, l, 2.0f / 512.0f);
  make_tw(t2, l & 7, 2.0f / 64.0f);
  f2* Lp = lds + p * WSTR;
#pragma unroll
  for (int i = 0; i < 4; ++i) {
    int k = (tid >> 2) + 64 * i;
    int kk = (512 - k) & 511;
    f4 z = o[i];
    if (k == 0) {
      Lp[0] = (f2){z.x, z.z};
      Lp[256 + 32] = (f2){z.y, z.w};
    } else {
      Lp[k + (k >> 3)] = (f2){z.x - z.w, z.y + z.z};
      Lp[kk + (kk >> 3)] = (f2){z.x + z.w, z.z - z.y};
    }
  }
  __syncthreads();
  f2* L = lds + w * WSTR;
  const int hi = l >> 3, lo = l & 7;
  f2 v[8];
#pragma unroll
  for (int k = 0; k < 8; ++k) v[k] = L[hi + 9 * lo + 72 * k];
  WAVE_BAR();
  dft8<true>(v);
#pragma unroll
  for (int n = 1; n < 8; ++n) v[n] = cmulc(v[n], t2[n]);
#pragma unroll
  for (int n = 0; n < 8; ++n) L[hi * 72 + lo * 9 + n] = v[n];
  WAVE_BAR();
#pragma unroll
  for (int k = 0; k < 8; ++k) v[k] = L[hi * 72 + k * 9 + lo];
  WAVE_BAR();
  dft8<true>(v);
#pragma unroll
  for (int n = 0; n < 8; ++n) L[hi * 72 + n * 8 + lo] = v[n];
  WAVE_BAR();
#pragma unroll
  for (int k = 0; k < 8; ++k) v[k] = L[k * 72 + l];
  WAVE_BAR();
#pragma unroll
  for (int k = 1; k < 8; ++k) v[k] = cmulc(v[k], t1[k]);
  dft8<true>(v);
  unsigned* lp = (unsigned*)L;
#pragma unroll
  for (int m = 0; m < 8; ++m) lp[l + 64 * m] = pack2(v[m].x, v[m].y);
  WAVE_BAR();
  u4 y0 = *(const u4*)(lp + l * 8), y1 = *(const u4*)(lp + l * 8 + 4);
  float a[8], c[8];
#pragma unroll
  for (int j = 0; j < 4; ++j) {
    unsigned ya = y0[j], yb = y1[j];
    float xa0 = (j & 1) ? bf2f_hi(ra[j >> 1]) : bf2f(ra[j >> 1] & 0xffffu);
    float xb0 = (j & 1) ? bf2f_hi(rb[j >> 1]) : bf2f(rb[j >> 1] & 0xffffu);
    float xa1 = (j & 1) ? bf2f_hi(ra[2 + (j >> 1)]) : bf2f(ra[2 + (j >> 1)] & 0xffffu);
    float xb1 = (j & 1) ? bf2f_hi(rb[2 + (j >> 1)]) : bf2f(rb[2 + (j >> 1)] & 0xffffu);
    a[j] = rbf(xa0 + bf2f(ya & 0xffffu));
    c[j] = rbf(xb0 + bf2f_hi(ya));
    a[j + 4] = rbf(xa1 + bf2f(yb & 0xffffu));
    c[j + 4] = rbf(xb1 + bf2f_hi(yb));
  }
  u4 ha, hb;
#pragma unroll
  for (int j = 0; j < 4; ++j) {
    ha[j] = (__float_as_uint(a[2 * j]) >> 16) | (__float_as_uint(a[2 * j + 1]) & 0xffff0000u);
    hb[j] = (__float_as_uint(c[2 * j]) >> 16) | (__float_as_uint(c[2 * j + 1]) & 0xffff0000u);
  }
  *(u4*)(h + (size_t)row * 512 + l * 8) = ha;
  *(u4*)(h + (size_t)(row + 1) * 512 + l * 8) = hb;
  ln_pair(a, c);
#pragma unroll
  for (int j = 0; j < 4; ++j) {
    ha[j] = (__float_as_uint(a[2 * j]) >> 16) | (__float_as_uint(a[2 * j + 1]) & 0xffff0000u);
    hb[j] = (__float_as_uint(c[2 * j]) >> 16) | (__float_as_uint(c[2 * j + 1]) & 0xffff0000u);
  }
  {
    int mt = row >> 7, rbk = (row >> 4) & 7, kb = l >> 2, ch = l & 3;
    size_t base = (((size_t)mt * 16 + kb) * 8 + rbk) * 64 + 16 * ch;
    *(u4*)(ln2 + (base + (row & 15)) * 8) = ha;
    *(u4*)(ln2 + (base + ((row + 1) & 15)) * 8) = hb;
  }
}

#define GLDS(gp, lp) __builtin_amdgcn_global_load_lds((const unsigned*)(gp), (unsigned*)(lp), 16, 0, 0)
#define WAIT_V(n) asm volatile("s_waitcnt vmcnt(" #n ")" ::: "memory")
#define RAW_BAR() do { asm volatile("s_waitcnt lgkmcnt(0)" ::: "memory"); __builtin_amdgcn_s_barrier(); } while (0)

__device__ __forceinline__ float gelu_t(float x) {
  float y2 = 1.5957691216057308f * (x + 0.044715f * x * x * x);
  return x / (1.0f + __expf(-y2));
}

__global__ __launch_bounds__(512) void k4_gemm1(const us* __restrict__ ln2, const us* __restrict__ W1t,
                                                us* __restrict__ act) {
  __shared__ __attribute__((aligned(16))) char sm[3 * 40960];
  const int tid = threadIdx.x, wid = tid >> 6, lane = tid & 63;
  const int xcd = blockIdx.x & 7, idx = blockIdx.x >> 3;
  const int mt = (xcd >> 1) * 4 + (idx >> 3), ft = (xcd & 1) * 8 + (idx & 7);
  const int wm = wid >> 1, wn = wid & 1;
  const char* gA = (const char*)ln2 + (size_t)mt * 131072 + tid * 16;
  const char* gB = (const char*)W1t + (size_t)ft * 196608 + tid * 16;
  auto stage = [&](int s, int buf) {
    char* dst = sm + buf * 40960 + wid * 1024;
    GLDS(gA + s * 16384, dst);
    GLDS(gA + s * 16384 + 8192, dst + 8192);
    GLDS(gB + s * 24576, dst + 16384);
    GLDS(gB + s * 24576 + 8192, dst + 16384 + 8192);
    GLDS(gB + s * 24576 + 16384, dst + 16384 + 16384);
  };
  f4 ag[2][3], au[2][3];
#pragma unroll
  for (int i = 0; i < 2; ++i)
#pragma unroll
    for (int f = 0; f < 3; ++f) { ag[i][f] = (f4){0, 0, 0, 0}; au[i][f] = (f4){0, 0, 0, 0}; }
  stage(0, 0);
  stage(1, 1);
#pragma unroll
  for (int s = 0; s < 8; ++s) {
    if (s < 7) WAIT_V(5); else WAIT_V(0);
    RAW_BAR();
    if (s + 2 < 8) stage(s + 2, (s + 2) % 3);
    const char* src = sm + (s % 3) * 40960 + lane * 16;
#pragma unroll
    for (int kk = 0; kk < 2; ++kk) {
      bf8 xb[2], wgf[3], wuf[3];
#pragma unroll
      for (int i = 0; i < 2; ++i) xb[i] = *(const bf8*)(src + (kk * 8 + wm * 2 + i) * 1024);
#pragma unroll
      for (int f = 0; f < 3; ++f) {
        wgf[f] = *(const bf8*)(src + 16384 + (kk * 12 + wn * 3 + f) * 1024);
        wuf[f] = *(const bf8*)(src + 16384 + (kk * 12 + 6 + wn * 3 + f) * 1024);
      }
#pragma unroll
      for (int i = 0; i < 2; ++i)
#pragma unroll
        for (int f = 0; f < 3; ++f) {
          ag[i][f] = __builtin_amdgcn_mfma_f32_16x16x32_bf16(wgf[f], xb[i], ag[i][f], 0, 0, 0);
          au[i][f] = __builtin_amdgcn_mfma_f32_16x16x32_bf16(wuf[f], xb[i], au[i][f], 0, 0, 0);
        }
    }
  }
#pragma unroll
  for (int i = 0; i < 2; ++i) {
    int row = mt * 128 + wm * 32 + i * 16 + (lane & 15);
    int mt2 = row >> 6, rbl = (row >> 4) & 3;
#pragma unroll
    for (int f = 0; f < 3; ++f) {
      int c = wn * 48 + f * 16 + 4 * (lane >> 4);
      float r[4];
#pragma unroll
      for (int j = 0; j < 4; ++j) {
        float gg = rbf(ag[i][f][j]), uu = rbf(au[i][f][j]);
        r[j] = rbf(gelu_t(gg)) * uu;
      }
      u2 o = (u2){pack2(r[0], r[1]), pack2(r[2], r[3])};
      int kbg = 3 * ft + (c >> 5), ch = (c & 31) >> 3;
      size_t off = ((((size_t)mt2 * 48 + kbg) * 4 + rbl) * 64 + (lane & 15) + 16 * ch) * 8 + (c & 7);
      *(u2*)(act + off) = o;
    }
  }
}

__global__ __launch_bounds__(512) void k5_gemm2(const us* __restrict__ act, const us* __restrict__ W2t,
                                                const us* __restrict__ h, us* __restrict__ out) {
  __shared__ __attribute__((aligned(16))) f4 red[8 * 16 * 64];
  const int tid = threadIdx.x, wid = tid >> 6, lane = tid & 63;
  const int xcd = blockIdx.x & 7, idx = blockIdx.x >> 3;
  const int mt2 = xcd * 4 + (idx >> 3), nt = idx & 7;
  const bf8* gA = (const bf8*)act + ((size_t)(mt2 * 48 + wid * 6) * 4) * 64 + lane;
  const bf8* gB = (const bf8*)W2t + ((size_t)(nt * 48 + wid * 6) * 4) * 64 + lane;
  f4 acc[4][4];
#pragma unroll
  for (int i = 0; i < 4; ++i)
#pragma unroll
    for (int j = 0; j < 4; ++j) acc[i][j] = (f4){0, 0, 0, 0};
#pragma unroll
  for (int kb = 0; kb < 6; ++kb) {
    bf8 xa[4], wb[4];
#pragma unroll
    for (int i = 0; i < 4; ++i) { xa[i] = gA[(kb * 4 + i) * 64]; wb[i] = gB[(kb * 4 + i) * 64]; }
#pragma unroll
    for (int nb = 0; nb < 4; ++nb)
#pragma unroll
      for (int rb = 0; rb < 4; ++rb)
        acc[nb][rb] = __builtin_amdgcn_mfma_f32_16x16x32_bf16(wb[nb], xa[rb], acc[nb][rb], 0, 0, 0);
  }
#pragma unroll
  for (int nb = 0; nb < 4; ++nb)
#pragma unroll
    for (int rb = 0; rb < 4; ++rb) red[(wid * 16 + nb * 4 + rb) * 64 + lane] = acc[nb][rb];
  __syncthreads();
  const int rb = wid >> 1;
#pragma unroll
  for (int q = 0; q < 2; ++q) {
    int nb = 2 * (wid & 1) + q;
    f4 s = red[(nb * 4 + rb) * 64 + lane];
#pragma unroll
    for (int src = 1; src < 8; ++src) s += red[(src * 16 + nb * 4 + rb) * 64 + lane];
    int token = mt2 * 64 + rb * 16 + (lane & 15);
    int col = nt * 64 + nb * 16 + 4 * (lane >> 4);
    u2 hv = *(const u2*)(h + (size_t)token * 512 + col);
    float o0 = bf2f(hv[0] & 0xffffu) + rbf(s[0]);
    float o1 = bf2f_hi(hv[0]) + rbf(s[1]);
    float o2 = bf2f(hv[1] & 0xffffu) + rbf(s[2]);
    float o3 = bf2f_hi(hv[1]) + rbf(s[3]);
    *(u2*)(out + (size_t)token * 512 + col) = (u2){pack2(o0, o1), pack2(o2, o3)};
  }
}

extern "C" void kernel_launch(void* const* d_in, const int* in_sizes, int n_in, void* d_out, int out_size,
                              void* d_ws, size_t ws_size, hipStream_t stream) {
  const us* x = (const us*)d_in[0];
  const us* wr = (const us*)d_in[1];
  const us* wi = (const us*)d_in[2];
  const us* wg = (const us*)d_in[4];
  const us* wu = (const us*)d_in[5];
  const us* wd = (const us*)d_in[6];
  char* ws = (char*)d_ws;
  f2* X1 = (f2*)(ws);
  unsigned* Wt = (unsigned*)(ws + (4u << 20));
  f4* Wsp = (f4*)(ws + (6u << 20));
  us* h = (us*)(ws + (7u << 20));
  us* ln2 = (us*)(ws + (9u << 20));
  us* act = (us*)(ws + (11u << 20));
  us* W1t = (us*)(ws + (17u << 20));
  us* W2t = (us*)(ws + (20u << 20));
  k1_lnfft<<<256 + 128 + 8 + 288, 256, 0, stream>>>(x, wr, wi, wg, wu, wd, X1, Wt, Wsp, W1t, W2t);
  k2_col<<<256, 256, 0, stream>>>(X1, Wt, Wsp);
  k3_ifft<<<256, 256, 0, stream>>>(X1, x, h, ln2);
  k4_gemm1<<<256, 512, 0, stream>>>(ln2, W1t, act);
  k5_gemm2<<<256, 512, 0, stream>>>(act, W2t, h, (us*)d_out);
}
```

```cpp
#include <hip/hip_runtime.h>
#include <hip/hip_bf16.h>
#include <stdint.h>

typedef float f2 __attribute__((ext_vector_type(2)));
typedef float f4 __attribute__((ext_vector_type(4)));
typedef unsigned u4 __attribute__((ext_vector_type(4)));
typedef unsigned u2 __attribute__((ext_vector_type(2)));
typedef short bf8 __attribute__((ext_vector_type(8)));
typedef unsigned short us;

#define WAVE_BAR() __builtin_amdgcn_wave_barrier()
#define STAMP_DECL
#define STAMP(i)
#define STAMPW(i)
#define STAMP_FLUSH(kid)

__device__ __forceinline__ float bf2f(unsigned h) { return __uint_as_float(h << 16); }
__device__ __forceinline__ float bf2f_hi(unsigned h) { return __uint_as_float(h & 0xffff0000u); }
__device__ __forceinline__ unsigned f2bf(float f) {
  __bf16 b = (__bf16)f;
  return (unsigned)__builtin_bit_cast(unsigned short, b);
}
__device__ __forceinline__ float rbf(float f) { return bf2f(f2bf(f)); }
__device__ __forceinline__ unsigned pack2(float lo, float hi) { return f2bf(lo) | (f2bf(hi) << 16); }

__device__ __forceinline__ f2 cmul(f2 a, f2 b) { return (f2){a.x * b.x - a.y * b.y, a.x * b.y + a.y * b.x}; }
__device__ __forceinline__ f2 cmulc(f2 a, f2 b) { return (f2){a.x * b.x + a.y * b.y, a.y * b.x - a.x * b.y}; }
template <bool INV> __device__ __forceinline__ f2 rot(f2 a) { return INV ? (f2){-a.y, a.x} : (f2){a.y, -a.x}; }

template <bool INV> __device__ __forceinline__ void dft8(f2 (&v)[8]) {
  const float r = 0.70710678118654752f;
  f2 a0 = v[0] + v[4], a1 = v[0] - v[4], a2 = v[2] + v[6], a3 = rot<INV>(v[2] - v[6]);
  f2 a4 = v[1] + v[5], a5 = v[1] - v[5], a6 = v[3] + v[7], a7 = rot<INV>(v[3] - v[7]);
  f2 e0 = a0 + a2, e2 = a0 - a2, e1 = a1 + a3, e3 = a1 - a3;
  f2 o0 = a4 + a6, o2 = a4 - a6, o1 = a5 + a7, o3 = a5 - a7;
  o1 = (o1 + rot<INV>(o1)) * r;
  o2 = rot<INV>(o2);
  o3 = (rot<INV>(o3) - o3) * r;
  v[0] = e0 + o0; v[4] = e0 - o0;
  v[1] = e1 + o1; v[5] = e1 - o1;
  v[2] = e2 + o2; v[6] = e2 - o2;
  v[3] = e3 + o3; v[7] = e3 - o3;
}
template <bool INV> __device__ __forceinline__ void dft4(f2& x0, f2& x1, f2& x2, f2& x3) {
  f2 a0 = x0 + x2, a1 = x0 - x2, a2 = x1 + x3, a3 = rot<INV>(x1 - x3);
  x0 = a0 + a2; x2 = a0 - a2; x1 = a1 + a3; x3 = a1 - a3;
}
__device__ __forceinline__ void make_tw(f2 (&t)[8], int num, float c2) {
  float s, c;
  sincospif(-(float)num * c2, &s, &c);
  t[1] = (f2){c, s};
  t[2] = cmul(t[1], t[1]);
  t[3] = cmul(t[2], t[1]);
  t[4] = cmul(t[2], t[2]);
  t[5] = cmul(t[4], t[1]);
  t[6] = cmul(t[3], t[3]);
  t[7] = cmul(t[4], t[3]);
}

template <int ctrl, int rm> __device__ __forceinline__ float dpp_add(float v) {
  int t = __builtin_amdgcn_update_dpp(0, __float_as_int(v), ctrl, rm, 0xf, false);
  return v + __int_as_float(t);
}
__device__ __forceinline__ float wave_sum(float v) {
  v = dpp_add<0x111, 0xf>(v);
  v = dpp_add<0x112, 0xf>(v);
  v = dpp_add<0x114, 0xf>(v);
  v = dpp_add<0x118, 0xf>(v);
  v = dpp_add<0x142, 0xa>(v);
  v = dpp_add<0x143, 0xc>(v);
  return __int_as_float(__builtin_amdgcn_readlane(__float_as_int(v), 63));
}

__device__ __forceinline__ void ln_pair(float (&a)[8], float (&b)[8]) {
  float sa = 0.f, sb = 0.f;
#pragma unroll
  for (int j = 0; j < 8; ++j) { sa += a[j]; sb += b[j]; }
  sa = wave_sum(sa); sb = wave_sum(sb);
  float ma = sa * (1.0f / 512.0f), mb = sb * (1.0f / 512.0f);
  float va = 0.f, vb = 0.f;
#pragma unroll
  for (int j = 0; j < 8; ++j) { float d = a[j] - ma; va += d * d; float e = b[j] - mb; vb += e * e; }
  va = wave_sum(va) * (1.0f / 512.0f); vb = wave_sum(vb) * (1.0f / 512.0f);
  float mra = rbf(ma), mrb = rbf(mb);
  float da = rbf(sqrtf(rbf(rbf(va) + 1e-5f))), db = rbf(sqrtf(rbf(rbf(vb) + 1e-5f)));
#pragma unroll
  for (int j = 0; j < 8; ++j) { a[j] = rbf(rbf(a[j] - mra) / da); b[j] = rbf(rbf(b[j] - mrb) / db); }
}

__device__ __forceinline__ int p_of_q(int q) {
  int T = q >> 3, e = q & 7;
  int k0 = T >> 5, k1 = (T >> 2) & 7, k2 = 2 * (T & 3) + (e >> 2), k3 = e & 3;
  return k0 + 8 * k1 + 64 * k2 + 512 * k3;
}

#define WSTR 584

__global__ __launch_bounds__(256) void k1_lnfft(const us* __restrict__ x, f2* __restrict__ X1) {
  __shared__ __attribute__((aligned(16))) f2 lds[4 * WSTR];
  const int b = (blockIdx.x & 7) * 32 + (blockIdx.x >> 3), tid = threadIdx.x;
  const int w = tid >> 6, l = tid & 63;
  STAMP_DECL; STAMP(0);
  const int row = b * 8 + w * 2;
  u4 ra = *(const u4*)(x + (size_t)row * 512 + l * 8);
  u4 rb = *(const u4*)(x + (size_t)(row + 1) * 512 + l * 8);
  f2 t1[8], t2[8];
  make_tw(t1, l, 2.0f / 512.0f);
  make_tw(t2, l & 7, 2.0f / 64.0f);
  STAMP(1); STAMPW(2);
  float a[8], c[8];
#pragma unroll
  for (int j = 0; j < 4; ++j) {
    a[2 * j] = bf2f(ra[j] & 0xffffu); a[2 * j + 1] = bf2f_hi(ra[j]);
    c[2 * j] = bf2f(rb[j] & 0xffffu); c[2 * j + 1] = bf2f_hi(rb[j]);
  }
  ln_pair(a, c);
  STAMP(3);
  f2* L = lds + w * WSTR;
  unsigned* lp = (unsigned*)L;
  u4 p0, p1;
#pragma unroll
  for (int j = 0; j < 4; ++j) {
    p0[j] = (__float_as_uint(a[j]) >> 16) | (__float_as_uint(c[j]) & 0xffff0000u);
    p1[j] = (__float_as_uint(a[j + 4]) >> 16) | (__float_as_uint(c[j + 4]) & 0xffff0000u);
  }
  *(u4*)(lp + l * 8) = p0;
  *(u4*)(lp + l * 8 + 4) = p1;
  WAVE_BAR();
  f2 v[8];
#pragma unroll
  for (int m = 0; m < 8; ++m) { unsigned q = lp[l + 64 * m]; v[m] = (f2){bf2f(q & 0xffffu), bf2f_hi(q)}; }
  WAVE_BAR();
  dft8<false>(v);
#pragma unroll
  for (int k = 1; k < 8; ++k) v[k] = cmul(v[k], t1[k]);
#pragma unroll
  for (int k = 0; k < 8; ++k) L[k * 72 + l] = v[k];
  WAVE_BAR();
  const int hi = l >> 3, lo = l & 7;
#pragma unroll
  for (int n = 0; n < 8; ++n) v[n] = L[hi * 72 + n * 8 + lo];
  WAVE_BAR();
  dft8<false>(v);
#pragma unroll
  for (int k = 1; k < 8; ++k) v[k] = cmul(v[k], t2[k]);
#pragma unroll
  for (int k = 0; k < 8; ++k) L[hi * 72 + k * 9 + lo] = v[k];
  WAVE_BAR();
#pragma unroll
  for (int n = 0; n < 8; ++n) v[n] = L[hi * 72 + lo * 9 + n];
  WAVE_BAR();
  dft8<false>(v);
#pragma unroll
  for (int k = 0; k < 8; ++k) L[hi + 9 * lo + 72 * k] = v[k];
  STAMP(4);
  __syncthreads();
  STAMP(5);
  const int p = tid & 3;
  const f2* Lp = lds + p * WSTR;
#pragma unroll
  for (int i = 0; i < 4; ++i) {
    int k = (tid >> 2) + 64 * i;
    int kk = (512 - k) & 511;
    f2 z = Lp[k + (k >> 3)];
    f2 y = Lp[kk + (kk >> 3)];
    f4 o = (f4){0.5f * (z.x + y.x), 0.5f * (z.y - y.y), 0.5f * (z.y + y.y), 0.5f * (y.x - z.x)};
    *(f4*)(X1 + (size_t)k * 2048 + b * 8 + 2 * p) = o;
  }
  if (tid < 4) {
    f2 z = Lp[256 + 32];
    *(f4*)(X1 + (size_t)256 * 2048 + b * 8 + 2 * p) = (f4){z.x, 0.f, z.y, 0.f};
  }
  STAMP(6);
  STAMP_FLUSH(0);
}

__global__ __launch_bounds__(256) void k2_col(const f2* __restrict__ X1, f2* __restrict__ X2,
                                              const us* __restrict__ wr, const us* __restrict__ wi) {
  __shared__ __attribute__((aligned(16))) f2 lds[8 * 288];
  const int c = blockIdx.x < 256 ? (blockIdx.x & 7) * 32 + (blockIdx.x >> 3) : 256;
  const int t = threadIdx.x;
  const f2* col = X1 + (size_t)c * 2048;
  STAMP_DECL; STAMP(0);
  f2 v[8];
#pragma unroll
  for (int m = 0; m < 8; ++m) v[m] = col[t + 256 * m];
  us wrv[8], wiv[8];
#pragma unroll
  for (int e = 0; e < 8; ++e) {
    int pp = p_of_q(t * 8 + e);
    wrv[e] = wr[pp * 257 + c];
    wiv[e] = wi[pp * 257 + c];
  }
  f2 t1[8], t2[8], t3[8];
  make_tw(t1, t, 2.0f / 2048.0f);
  make_tw(t2, t & 31, 2.0f / 256.0f);
  make_tw(t3, t & 3, 2.0f / 32.0f);
  STAMP(1);
  const int g = t >> 5, lo5 = t & 31;
  f2* R = lds + g * 288;
  dft8<false>(v);
#pragma unroll
  for (int k = 1; k < 8; ++k) v[k] = cmul(v[k], t1[k]);
#pragma unroll
  for (int k = 0; k < 8; ++k) lds[k * 288 + t] = v[k];
  STAMP(2);
  __syncthreads();
#pragma unroll
  for (int n = 0; n < 8; ++n) v[n] = R[n * 32 + lo5];
  WAVE_BAR();
  dft8<false>(v);
#pragma unroll
  for (int k = 1; k < 8; ++k) v[k] = cmul(v[k], t2[k]);
#pragma unroll
  for (int k = 0; k < 8; ++k) R[k * 36 + lo5] = v[k];
  WAVE_BAR();
  const int m3 = (t >> 2) & 7, q3 = t & 3;
#pragma unroll
  for (int n = 0; n < 8; ++n) v[n] = R[m3 * 36 + 4 * n + q3];
  WAVE_BAR();
  dft8<false>(v);
#pragma unroll
  for (int k = 1; k < 8; ++k) v[k] = cmul(v[k], t3[k]);
#pragma unroll
  for (int k = 0; k < 8; ++k) R[m3 * 34 + 4 * k + q3] = v[k];
  WAVE_BAR();
#pragma unroll
  for (int e = 0; e < 8; ++e) v[e] = R[m3 * 34 + 8 * q3 + e];
  WAVE_BAR();
  dft4<false>(v[0], v[1], v[2], v[3]);
  dft4<false>(v[4], v[5], v[6], v[7]);
  STAMP(3);
  {
    const float sc = 1.0f / 1048576.0f;
#pragma unroll
    for (int e = 0; e < 8; ++e) v[e] = cmul(v[e], (f2){bf2f(wrv[e]) * sc, bf2f(wiv[e]) * sc});
  }
  STAMP(4);
  dft4<true>(v[0], v[1], v[2], v[3]);
  dft4<true>(v[4], v[5], v[6], v[7]);
#pragma unroll
  for (int e = 0; e < 8; ++e) R[m3 * 34 + 8 * q3 + e] = v[e];
  WAVE_BAR();
#pragma unroll
  for (int k = 0; k < 8; ++k) v[k] = R[m3 * 34 + 4 * k + q3];
  WAVE_BAR();
#pragma unroll
  for (int k = 1; k < 8; ++k) v[k] = cmulc(v[k], t3[k]);
  dft8<true>(v);
#pragma unroll
  for (int n = 0; n < 8; ++n) R[m3 * 36 + 4 * n + q3] = v[n];
  WAVE_BAR();
#pragma unroll
  for (int k = 0; k < 8; ++k) v[k] = R[k * 36 + lo5];
  WAVE_BAR();
#pragma unroll
  for (int k = 1; k < 8; ++k) v[k] = cmulc(v[k], t2[k]);
  dft8<true>(v);
#pragma unroll
  for (int n = 0; n < 8; ++n) R[n * 32 + lo5] = v[n];
  __syncthreads();
#pragma unroll
  for (int k = 0; k < 8; ++k) v[k] = lds[k * 288 + t];
#pragma unroll
  for (int k = 1; k < 8; ++k) v[k] = cmulc(v[k], t1[k]);
  dft8<true>(v);
  STAMP(5);
  f2* ocol = X2 + (size_t)c * 2048;
#pragma unroll
  for (int m = 0; m < 8; ++m) ocol[t + 256 * m] = v[m];
  STAMP(6);
  STAMP_FLUSH(1);
}

__global__ __launch_bounds__(256) void k3_ifft(const f2* __restrict__ X2, const us* __restrict__ x,
                                               us* __restrict__ h, us* __restrict__ ln2,
                                               const us* __restrict__ wg, const us* __restrict__ wu) {
  __shared__ __attribute__((aligned(16))) f2 lds[4 * WSTR];
  const int tid = threadIdx.x;
  if (blockIdx.x >= 256) {
    const int j = (blockIdx.x - 256) >> 3;
    const char* pg = (const char*)wg + j * 88064 + tid * 16;
    const char* pu = (const char*)wu + j * 88064 + tid * 16;
    u4 acc = {0, 0, 0, 0};
#pragma unroll
    for (int i = 0; i < 21; ++i) { acc ^= *(const u4*)(pg + i * 4096); acc ^= *(const u4*)(pu + i * 4096); }
    if (tid < 128) { acc ^= *(const u4*)(pg + 21 * 4096); acc ^= *(const u4*)(pu + 21 * 4096); }
    asm volatile("" ::"v"(acc));
    return;
  }
  const int b = (blockIdx.x & 7) * 32 + (blockIdx.x >> 3);
  const int w = tid >> 6, l = tid & 63;
  const int p = tid & 3;
  STAMP_DECL; STAMP(0);
  f4 o[4];
#pragma unroll
  for (int i = 0; i < 4; ++i) {
    int k = (tid >> 2) + 64 * i;
    o[i] = *(const f4*)(X2 + (size_t)k * 2048 + b * 8 + 2 * p);
  }
  f4 ony = *(const f4*)(X2 + (size_t)256 * 2048 + b * 8 + 2 * p);
  const int row = b * 8 + w * 2;
  u4 ra = *(const u4*)(x + (size_t)row * 512 + l * 8);
  u4 rb = *(const u4*)(x + (size_t)(row + 1) * 512 + l * 8);
  f2 t1[8], t2[8];
  make_tw(t1, l, 2.0f / 512.0f);
  make_tw(t2, l & 7, 2.0f / 64.0f);
  STAMP(1); STAMPW(2);
  f2* Lp = lds + p * WSTR;
#pragma unroll
  for (int i = 0; i < 4; ++i) {
    int k = (tid >> 2) + 64 * i;
    int kk = (512 - k) & 511;
    f4 z = o[i];
    if (k == 0) {
      Lp[0] = (f2){z.x, z.z};
      Lp[256 + 32] = (f2){ony.x, ony.z};
    } else {
      Lp[k + (k >> 3)] = (f2){z.x - z.w, z.y + z.z};
      Lp[kk + (kk >> 3)] = (f2){z.x + z.w, z.z - z.y};
    }
  }
  __syncthreads();
  STAMP(3);
  f2* L = lds + w * WSTR;
  const int hi = l >> 3, lo = l & 7;
  f2 v[8];
#pragma unroll
  for (int k = 0; k < 8; ++k) v[k] = L[hi + 9 * lo + 72 * k];
  WAVE_BAR();
  dft8<true>(v);
#pragma unroll
  for (int n = 1; n < 8; ++n) v[n] = cmulc(v[n], t2[n]);
#pragma unroll
  for (int n = 0; n < 8; ++n) L[hi * 72 + lo * 9 + n] = v[n];
  WAVE_BAR();
#pragma unroll
  for (int k = 0; k < 8; ++k) v[k] = L[hi * 72 + k * 9 + lo];
  WAVE_BAR();
  dft8<true>(v);
#pragma unroll
  for (int n = 0; n < 8; ++n) L[hi * 72 + n * 8 + lo] = v[n];
  WAVE_BAR();
#pragma unroll
  for (int k = 0; k < 8; ++k) v[k] = L[k * 72 + l];
  WAVE_BAR();
#pragma unroll
  for (int k = 1; k < 8; ++k) v[k] = cmulc(v[k], t1[k]);
  dft8<true>(v);
  STAMP(4);
  unsigned* lp = (unsigned*)L;
#pragma unroll
  for (int m = 0; m < 8; ++m) lp[l + 64 * m] = pack2(v[m].x, v[m].y);
  WAVE_BAR();
  u4 y0 = *(const u4*)(lp + l * 8), y1 = *(const u4*)(lp + l * 8 + 4);
  float a[8], c[8];
#pragma unroll
  for (int j = 0; j < 4; ++j) {
    unsigned ya = y0[j], yb = y1[j];
    float xa0 = (j & 1) ? bf2f_hi(ra[j >> 1]) : bf2f(ra[j >> 1] & 0xffffu);
    float xb0 = (j & 1) ? bf2f_hi(rb[j >> 1]) : bf2f(rb[j >> 1] & 0xffffu);
    float xa1 = (j & 1) ? bf2f_hi(ra[2 + (j >> 1)]) : bf2f(ra[2 + (j >> 1)] & 0xffffu);
    float xb1 = (j & 1) ? bf2f_hi(rb[2 + (j >> 1)]) : bf2f(rb[2 + (j >> 1)] & 0xffffu);
    a[j] = rbf(xa0 + bf2f(ya & 0xffffu));
    c[j] = rbf(xb0 + bf2f_hi(ya));
    a[j + 4] = rbf(xa1 + bf2f(yb & 0xffffu));
    c[j + 4] = rbf(xb1 + bf2f_hi(yb));
  }
  u4 ha, hb;
#pragma unroll
  for (int j = 0; j < 4; ++j) {
    ha[j] = (__float_as_uint(a[2 * j]) >> 16) | (__float_as_uint(a[2 * j + 1]) & 0xffff0000u);
    hb[j] = (__float_as_uint(c[2 * j]) >> 16) | (__float_as_uint(c[2 * j + 1]) & 0xffff0000u);
  }
  *(u4*)(h + (size_t)row * 512 + l * 8) = ha;
  *(u4*)(h + (size_t)(row + 1) * 512 + l * 8) = hb;
  ln_pair(a, c);
#pragma unroll
  for (int j = 0; j < 4; ++j) {
    ha[j] = (__float_as_uint(a[2 * j]) >> 16) | (__float_as_uint(a[2 * j + 1]) & 0xffff0000u);
    hb[j] = (__float_as_uint(c[2 * j]) >> 16) | (__float_as_uint(c[2 * j + 1]) & 0xffff0000u);
  }
  {
    int mt = row >> 7, rbk = (row >> 4) & 7, kb = l >> 2, ch = l & 3;
    size_t base = (((size_t)mt * 16 + kb) * 8 + rbk) * 64 + 16 * ch;
    *(u4*)(ln2 + (base + (row & 15)) * 8) = ha;
    *(u4*)(ln2 + (base + ((row + 1) & 15)) * 8) = hb;
  }
  STAMP(5);
  STAMP_FLUSH(2);
}

#define GLDS(gp, lp) __builtin_amdgcn_global_load_lds((const unsigned*)(gp), (unsigned*)(lp), 16, 0, 0)
#define WAIT_V(n) asm volatile("s_waitcnt vmcnt(" #n ")" ::: "memory")
#define WAIT_L0() asm volatile("s_waitcnt lgkmcnt(0)" ::: "memory")
#define RAW_BAR() do { asm volatile("s_waitcnt lgkmcnt(0)" ::: "memory"); __builtin_amdgcn_s_barrier(); } while (0)
typedef short s4v __attribute__((ext_vector_type(4)));
__device__ __forceinline__ s4v tr_read(const char* p) {
  return __builtin_amdgcn_ds_read_tr16_b64_v4i16((__attribute__((address_space(3))) s4v*)p);
}
__device__ __forceinline__ bf8 tr_frag(const char* p, int pitch4) {
  s4v a = tr_read(p), b = tr_read(p + pitch4);
  return (bf8){a[0], a[1], a[2], a[3], b[0], b[1], b[2], b[3]};
}

__device__ __forceinline__ float gelu_t(float x) {
  float y2 = -2.3022082007793f * (x + 0.044715f * x * x * x);
  return x * __builtin_amdgcn_rcpf(1.0f + __builtin_amdgcn_exp2f(y2));
}

__global__ __launch_bounds__(512) void k4_gemm1(const us* __restrict__ ln2, const us* __restrict__ wg,
                                                const us* __restrict__ wu, const us* __restrict__ wd,
                                                us* __restrict__ act) {
  __shared__ __attribute__((aligned(16))) char sm[3 * 40960 + 8192];
  const int tid = threadIdx.x, wid = tid >> 6, lane = tid & 63;
  const int xcd = blockIdx.x & 7, idx = blockIdx.x >> 3;
  const int mt = 2 * xcd + (idx >> 4), ft = idx & 15;
  const int wm = wid >> 1, wn = wid & 1;
  STAMP_DECL; STAMP(0);
  const char* gA = (const char*)ln2 + (size_t)mt * 131072 + tid * 16;
  const char* gB[3];
#pragma unroll
  for (int i = 0; i < 3; ++i) {
    int q = i * 512 + tid;
    int mat = q >= 768 ? 1 : 0;
    int qq = q - mat * 768;
    int r = qq / 12, cp = qq - r * 12;
    int cch = cp ^ (((r >> 3) & 1) << 1);
    int col0 = 88 * ft + 8 * cch;
    col0 = col0 > 1368 ? 1368 : col0;
    gB[i] = (const char*)((mat ? wu : wg) + (size_t)r * 1376 + col0);
  }
  auto stage = [&](int s, int buf) {
    char* dst = sm + buf * 40960 + wid * 1024;
    GLDS(gA + s * 16384, dst);
    GLDS(gA + s * 16384 + 8192, dst + 8192);
    GLDS(gB[0] + (size_t)s * 64 * 2752, dst + 16384);
    GLDS(gB[1] + (size_t)s * 64 * 2752, dst + 16384 + 8192);
    GLDS(gB[2] + (size_t)s * 64 * 2752, dst + 16384 + 16384);
  };
  f4 ag[2][3], au[2][3];
#pragma unroll
  for (int i = 0; i < 2; ++i)
#pragma unroll
    for (int f = 0; f < 3; ++f) { ag[i][f] = (f4){0, 0, 0, 0}; au[i][f] = (f4){0, 0, 0, 0}; }
  stage(0, 0);
  stage(1, 1);
  {
    const char* pd = (const char*)wd + (size_t)idx * 44032 + tid * 16;
    char* dump = sm + 3 * 40960 + wid * 1024;
#pragma unroll
    for (int i = 0; i < 5; ++i) GLDS(pd + i * 8192, dump);
    GLDS(pd + (tid < 192 ? 5 * 8192 : 0), dump);
  }
  const int g = lane >> 4, q4 = (lane & 15) >> 2, p4 = lane & 3;
  const int tb = (8 * g + q4) * 192 + 8 * (p4 & 1) + 16 * (p4 >> 1);
  const int tbe = tb + 32 * (g & 1), tbo = tb - 32 * (g & 1);
#pragma unroll
  for (int s = 0; s < 8; ++s) {
    if (s < 2) WAIT_V(11); else if (s < 7) WAIT_V(5); else WAIT_V(0);
    RAW_BAR();
    STAMP(1 + s);
    if (s + 2 < 8) stage(s + 2, (s + 2) % 3);
    const char* srcA = sm + (s % 3) * 40960 + lane * 16;
    const char* srcB = sm + (s % 3) * 40960 + 16384;
#pragma unroll
    for (int kk = 0; kk < 2; ++kk) {
      bf8 xb[2], wgf[3], wuf[3];
#pragma unroll
      for (int i = 0; i < 2; ++i) xb[i] = *(const bf8*)(srcA + (kk * 8 + wm * 2 + i) * 1024);
#pragma unroll
      for (int f = 0; f < 3; ++f) {
        const int fi_e = wn * 3 + f;
        const char* base = srcB + kk * 32 * 192 + 32 * fi_e + ((fi_e & 1) ? tbo : tbe);
        wgf[f] = tr_frag(base, 4 * 192);
        wuf[f] = tr_frag(base + 64 * 192, 4 * 192);
      }
#pragma unroll
      for (int i = 0; i < 2; ++i)
#pragma unroll
        for (int f = 0; f < 3; ++f) {
          ag[i][f] = __builtin_amdgcn_mfma_f32_16x16x32_bf16(wgf[f], xb[i], ag[i][f], 0, 0, 0);
          au[i][f] = __builtin_amdgcn_mfma_f32_16x16x32_bf16(wuf[f], xb[i], au[i][f], 0, 0, 0);
        }
    }
  }
  STAMP(9);
#pragma unroll
  for (int i = 0; i < 2; ++i) {
    int row = mt * 128 + wm * 32 + i * 16 + (lane & 15);
    int mt2 = row >> 6, rbl = (row >> 4) & 3;
#pragma unroll
    for (int f = 0; f < 3; ++f) {
      int c = wn * 48 + f * 16 + 4 * (lane >> 4);
      int k = 88 * ft + c;
      float r[4];
#pragma unroll
      for (int j = 0; j < 4; ++j) {
        float gg = rbf(ag[i][f][j]), uu = rbf(au[i][f][j]);
        r[j] = rbf(gelu_t(gg)) * uu;
      }
      u2 o = (u2){pack2(r[0], r[1]), pack2(r[2], r[3])};
      size_t off = ((((size_t)mt2 * 43 + (k >> 5)) * 4 + rbl) * 64 + (lane & 15) + 16 * ((k & 31) >> 3)) * 8 + (k & 7);
      if (c < 88 && k < 1376) *(u2*)(act + off) = o;
    }
  }
  STAMP(10);
  STAMP_FLUSH(3);
}

__global__ __launch_bounds__(512) void k5_gemm2(const us* __restrict__ act, const us* __restrict__ wd,
                                                const us* __restrict__ h, us* __restrict__ out) {
  __shared__ __attribute__((aligned(16))) char sm[131072];
  const int tid = threadIdx.x, wid = tid >> 6, lane = tid & 63;
  const int xcd = blockIdx.x & 7, idx = blockIdx.x >> 3;
  const int mt2 = xcd * 4 + (idx >> 3), nt = idx & 7;
  STAMP_DECL; STAMP(0);
  char* ring = sm + wid * 16384;
  const char* gA = (const char*)act + ((size_t)mt2 * 43 * 4) * 1024 + lane * 16;
  const char* gW[4];
#pragma unroll
  for (int i = 0; i < 4; ++i) {
    int rho = 8 * i + (lane >> 3);
    int sw = ((rho >> 1) & 1) | (((rho >> 3) & 1) << 1);
    int ch = (lane & 7) ^ (sw << 1);
    gW[i] = (const char*)(wd + (size_t)rho * 512 + nt * 64 + ch * 8);
  }
  auto stage = [&](int kb, int buf) {
    char* dst = ring + buf * 8192;
#pragma unroll
    for (int i = 0; i < 4; ++i) GLDS(gA + (size_t)kb * 4096 + i * 1024, dst + i * 1024);
#pragma unroll
    for (int i = 0; i < 4; ++i) GLDS(gW[i] + (size_t)kb * 32 * 1024, dst + 4096 + i * 1024);
  };
  f4 acc[4][4];
#pragma unroll
  for (int i = 0; i < 4; ++i)
#pragma unroll
    for (int j = 0; j < 4; ++j) acc[i][j] = (f4){0, 0, 0, 0};
  const int g = lane >> 4, q4 = (lane & 15) >> 2, p4 = lane & 3;
  const int sw = ((q4 >> 1) & 1) | ((g & 1) << 1);
  const int trb = 4096 + (8 * g + q4) * 128 + 8 * (p4 & 1) + 16 * (p4 >> 1);
  const int nst = wid < 3 ? 6 : 5;
  stage(wid, 0);
  stage(wid + 8, 1);
#pragma unroll
  for (int i = 0; i < 6; ++i) {
    if (i < nst) {
      if (i + 1 < nst) WAIT_V(8); else WAIT_V(0);
      const char* src = ring + (i & 1) * 8192;
      bf8 xa[4], wb[4];
#pragma unroll
      for (int r = 0; r < 4; ++r) xa[r] = *(const bf8*)(src + r * 1024 + lane * 16);
#pragma unroll
      for (int f = 0; f < 4; ++f) wb[f] = tr_frag(src + trb + 32 * (f ^ sw), 4 * 128);
      WAIT_L0();
      __builtin_amdgcn_sched_barrier(0);
      if (i + 2 < nst) stage(wid + 8 * (i + 2), i & 1);
#pragma unroll
      for (int nb = 0; nb < 4; ++nb)
#pragma unroll
        for (int rb = 0; rb < 4; ++rb)
          acc[nb][rb] = __builtin_amdgcn_mfma_f32_16x16x32_bf16(wb[nb], xa[rb], acc[nb][rb], 0, 0, 0);
    }
  }
  STAMP(1);
  __syncthreads();
  f4* red = (f4*)sm;
#pragma unroll
  for (int nb = 0; nb < 4; ++nb)
#pragma unroll
    for (int rb = 0; rb < 4; ++rb) red[(wid * 16 + nb * 4 + rb) * 64 + lane] = acc[nb][rb];
  __syncthreads();
  STAMP(2);
  const int rb = wid >> 1;
#pragma unroll
  for (int q = 0; q < 2; ++q) {
    int nb = 2 * (wid & 1) + q;
    f4 s = red[(nb * 4 + rb) * 64 + lane];
#pragma unroll
    for (int src = 1; src < 8; ++src) s += red[(src * 16 + nb * 4 + rb) * 64 + lane];
    int token = mt2 * 64 + rb * 16 + (lane & 15);
    int col = nt * 64 + nb * 16 + 4 * (lane >> 4);
    u2 hv = *(const u2*)(h + (size_t)token * 512 + col);
    float o0 = bf2f(hv[0] & 0xffffu) + rbf(s[0]);
    float o1 = bf2f_hi(hv[0]) + rbf(s[1]);
    float o2 = bf2f(hv[1] & 0xffffu) + rbf(s[2]);
    float o3 = bf2f_hi(hv[1]) + rbf(s[3]);
    *(u2*)(out + (size_t)token * 512 + col) = (u2){pack2(o0, o1), pack2(o2, o3)};
  }
  STAMP(3);
  STAMP_FLUSH(4);
}

extern "C" void kernel_launch(void* const* d_in, const int* in_sizes, int n_in, void* d_out, int out_size,
                              void* d_ws, size_t ws_size, hipStream_t stream) {
  const us* x = (const us*)d_in[0];
  const us* wr = (const us*)d_in[1];
  const us* wi = (const us*)d_in[2];
  const us* wg = (const us*)d_in[4];
  const us* wu = (const us*)d_in[5];
  const us* wd = (const us*)d_in[6];
  char* ws = (char*)d_ws;
  f2* X1 = (f2*)(ws);
  f2* X2 = (f2*)(ws + (5u << 20));
  us* h = (us*)(ws + (10u << 20));
  us* ln2 = (us*)(ws + (12u << 20));
  us* act = (us*)(ws + (14u << 20));
  k1_lnfft<<<256, 256, 0, stream>>>(x, X1);
  k2_col<<<257, 256, 0, stream>>>(X1, X2, wr, wi);
  k3_ifft<<<256 + 128, 256, 0, stream>>>(X2, x, h, ln2, wg, wu);
  k4_gemm1<<<256, 512, 0, stream>>>(ln2, wg, wu, wd, act);
  k5_gemm2<<<256, 512, 0, stream>>>(act, wd, h, (us*)d_out);
}
```

```cpp
#include <hip/hip_runtime.h>
#include <hip/hip_bf16.h>
#include <stdint.h>

typedef float f2 __attribute__((ext_vector_type(2)));
typedef float f4 __attribute__((ext_vector_type(4)));
typedef unsigned u4 __attribute__((ext_vector_type(4)));
typedef unsigned u2 __attribute__((ext_vector_type(2)));
typedef short bf8 __attribute__((ext_vector_type(8)));
typedef unsigned short us;

#define WAVE_BAR() __builtin_amdgcn_wave_barrier()
#define STAMP_DECL
#define STAMP(i)
#define STAMPW(i)
#define STAMP_FLUSH(kid)

__device__ __forceinline__ float bf2f(unsigned h) { return __uint_as_float(h << 16); }
__device__ __forceinline__ float bf2f_hi(unsigned h) { return __uint_as_float(h & 0xffff0000u); }
__device__ __forceinline__ unsigned f2bf(float f) {
  __bf16 b = (__bf16)f;
  return (unsigned)__builtin_bit_cast(unsigned short, b);
}
__device__ __forceinline__ float rbf(float f) { return bf2f(f2bf(f)); }
__device__ __forceinline__ unsigned pack2(float lo, float hi) { return f2bf(lo) | (f2bf(hi) << 16); }

__device__ __forceinline__ f2 cmul(f2 a, f2 b) { return (f2){a.x * b.x - a.y * b.y, a.x * b.y + a.y * b.x}; }
__device__ __forceinline__ f2 cmulc(f2 a, f2 b) { return (f2){a.x * b.x + a.y * b.y, a.y * b.x - a.x * b.y}; }
template <bool INV> __device__ __forceinline__ f2 rot(f2 a) { return INV ? (f2){-a.y, a.x} : (f2){a.y, -a.x}; }

template <bool INV> __device__ __forceinline__ void dft8(f2 (&v)[8]) {
  const float r = 0.70710678118654752f;
  f2 a0 = v[0] + v[4], a1 = v[0] - v[4], a2 = v[2] + v[6], a3 = rot<INV>(v[2] - v[6]);
  f2 a4 = v[1] + v[5], a5 = v[1] - v[5], a6 = v[3] + v[7], a7 = rot<INV>(v[3] - v[7]);
  f2 e0 = a0 + a2, e2 = a0 - a2, e1 = a1 + a3, e3 = a1 - a3;
  f2 o0 = a4 + a6, o2 = a4 - a6, o1 = a5 + a7, o3 = a5 - a7;
  o1 = (o1 + rot<INV>(o1)) * r;
  o2 = rot<INV>(o2);
  o3 = (rot<INV>(o3) - o3) * r;
  v[0] = e0 + o0; v[4] = e0 - o0;
  v[1] = e1 + o1; v[5] = e1 - o1;
  v[2] = e2 + o2; v[6] = e2 - o2;
  v[3] = e3 + o3; v[7] = e3 - o3;
}
template <bool INV> __device__ __forceinline__ void dft4(f2& x0, f2& x1, f2& x2, f2& x3) {
  f2 a0 = x0 + x2, a1 = x0 - x2, a2 = x1 + x3, a3 = rot<INV>(x1 - x3);
  x0 = a0 + a2; x2 = a0 - a2; x1 = a1 + a3; x3 = a1 - a3;
}
__device__ __forceinline__ void make_tw(f2 (&t)[8], int num, float c2) {
  float s, c;
  sincospif(-(float)num * c2, &s, &c);
  t[1] = (f2){c, s};
  t[2] = cmul(t[1], t[1]);
  t[3] = cmul(t[2], t[1]);
  t[4] = cmul(t[2], t[2]);
  t[5] = cmul(t[4], t[1]);
  t[6] = cmul(t[3], t[3]);
  t[7] = cmul(t[4], t[3]);
}

template <int ctrl, int rm> __device__ __forceinline__ float dpp_add(float v) {
  int t = __builtin_amdgcn_update_dpp(0, __float_as_int(v), ctrl, rm, 0xf, false);
  return v + __int_as_float(t);
}
__device__ __forceinline__ float wave_sum(float v) {
  v = dpp_add<0x111, 0xf>(v);
  v = dpp_add<0x112, 0xf>(v);
  v = dpp_add<0x114, 0xf>(v);
  v = dpp_add<0x118, 0xf>(v);
  v = dpp_add<0x142, 0xa>(v);
  v = dpp_add<0x143, 0xc>(v);
  return __int_as_float(__builtin_amdgcn_readlane(__float_as_int(v), 63));
}

__device__ __forceinline__ void ln_pair(float (&a)[8], float (&b)[8]) {
  float sa = 0.f, sb = 0.f;
#pragma unroll
  for (int j = 0; j < 8; ++j) { sa += a[j]; sb += b[j]; }
  sa = wave_sum(sa); sb = wave_sum(sb);
  float ma = sa * (1.0f / 512.0f), mb = sb * (1.0f / 512.0f);
  float va = 0.f, vb = 0.f;
#pragma unroll
  for (int j = 0; j < 8; ++j) { float d = a[j] - ma; va += d * d; float e = b[j] - mb; vb += e * e; }
  va = wave_sum(va) * (1.0f / 512.0f); vb = wave_sum(vb) * (1.0f / 512.0f);
  float mra = rbf(ma), mrb = rbf(mb);
  float da = rbf(sqrtf(rbf(rbf(va) + 1e-5f))), db = rbf(sqrtf(rbf(rbf(vb) + 1e-5f)));
#pragma unroll
  for (int j = 0; j < 8; ++j) { a[j] = rbf(rbf(a[j] - mra) / da); b[j] = rbf(rbf(b[j] - mrb) / db); }
}

__device__ __forceinline__ int p_of_q(int q) {
  int T = q >> 3, e = q & 7;
  int k0 = T >> 5, k1 = (T >> 2) & 7, k2 = 2 * (T & 3) + (e >> 2), k3 = e & 3;
  return k0 + 8 * k1 + 64 * k2 + 512 * k3;
}

#define WSTR 584

__global__ __launch_bounds__(256) void k1_lnfft(const us* __restrict__ x, f2* __restrict__ X1,
                                                const us* __restrict__ wr, const us* __restrict__ wi,
                                                unsigned* __restrict__ Wt) {
  __shared__ __attribute__((aligned(16))) f2 lds[4 * WSTR];
  const int tid = threadIdx.x;
  if (blockIdx.x >= 256) {
    unsigned* tile = (unsigned*)lds;
    const int xg = blockIdx.x & 7, qi = (blockIdx.x - 256) >> 3;
    const int c = tid & 31;
#pragma unroll
    for (int i = 0; i < 8; ++i) {
      int qq = (tid >> 5) + 8 * i;
      int pp = p_of_q(64 * qi + qq);
      unsigned r_ = wr[pp * 257 + 32 * xg + c], i_ = wi[pp * 257 + 32 * xg + c];
      tile[c * 65 + qq] = r_ | (i_ << 16);
    }
    if (xg == 0 && tid < 64) {
      int pp = p_of_q(64 * qi + tid);
      Wt[(size_t)256 * 2048 + 64 * qi + tid] = (unsigned)wr[pp * 257 + 256] | ((unsigned)wi[pp * 257 + 256] << 16);
    }
    __syncthreads();
#pragma unroll
    for (int i = 0; i < 8; ++i) {
      int cc = (tid >> 6) + 4 * i;
      Wt[(size_t)(32 * xg + cc) * 2048 + 64 * qi + (tid & 63)] = tile[cc * 65 + (tid & 63)];
    }
    return;
  }
  const int b = (blockIdx.x & 7) * 32 + (blockIdx.x >> 3);
  const int w = tid >> 6, l = tid & 63;
  STAMP_DECL; STAMP(0);
  const int row = b * 8 + w * 2;
  u4 ra = *(const u4*)(x + (size_t)row * 512 + l * 8);
  u4 rb = *(const u4*)(x + (size_t)(row + 1) * 512 + l * 8);
  f2 t1[8], t2[8];
  make_tw(t1, l, 2.0f / 512.0f);
  make_tw(t2, l & 7, 2.0f / 64.0f);
  STAMP(1); STAMPW(2);
  float a[8], c[8];
#pragma unroll
  for (int j = 0; j < 4; ++j) {
    a[2 * j] = bf2f(ra[j] & 0xffffu); a[2 * j + 1] = bf2f_hi(ra[j]);
    c[2 * j] = bf2f(rb[j] & 0xffffu); c[2 * j + 1] = bf2f_hi(rb[j]);
  }
  ln_pair(a, c);
  STAMP(3);
  f2* L = lds + w * WSTR;
  unsigned* lp = (unsigned*)L;
  u4 p0, p1;
#pragma unroll
  for (int j = 0; j < 4; ++j) {
    p0[j] = (__float_as_uint(a[j]) >> 16) | (__float_as_uint(c[j]) & 0xffff0000u);
    p1[j] = (__float_as_uint(a[j + 4]) >> 16) | (__float_as_uint(c[j + 4]) & 0xffff0000u);
  }
  *(u4*)(lp + l * 8) = p0;
  *(u4*)(lp + l * 8 + 4) = p1;
  WAVE_BAR();
  f2 v[8];
#pragma unroll
  for (int m = 0; m < 8; ++m) { unsigned q = lp[l + 64 * m]; v[m] = (f2){bf2f(q & 0xffffu), bf2f_hi(q)}; }
  WAVE_BAR();
  dft8<false>(v);
#pragma unroll
  for (int k = 1; k < 8; ++k) v[k] = cmul(v[k], t1[k]);
#pragma unroll
  for (int k = 0; k < 8; ++k) L[k * 72 + l] = v[k];
  WAVE_BAR();
  const int hi = l >> 3, lo = l & 7;
#pragma unroll
  for (int n = 0; n < 8; ++n) v[n] = L[hi * 72 + n * 8 + lo];
  WAVE_BAR();
  dft8<false>(v);
#pragma unroll
  for (int k = 1; k < 8; ++k) v[k] = cmul(v[k], t2[k]);
#pragma unroll
  for (int k = 0; k < 8; ++k) L[hi * 72 + k * 9 + lo] = v[k];
  WAVE_BAR();
#pragma unroll
  for (int n = 0; n < 8; ++n) v[n] = L[hi * 72 + lo * 9 + n];
  WAVE_BAR();
  dft8<false>(v);
#pragma unroll
  for (int k = 0; k < 8; ++k) L[hi + 9 * lo + 72 * k] = v[k];
  STAMP(4);
  __syncthreads();
  STAMP(5);
  const int p = tid & 3;
  const f2* Lp = lds + p * WSTR;
#pragma unroll
  for (int i = 0; i < 4; ++i) {
    int k = (tid >> 2) + 64 * i;
    int kk = (512 - k) & 511;
    f2 z = Lp[k + (k >> 3)];
    f2 y = Lp[kk + (kk >> 3)];
    f4 o = (f4){0.5f * (z.x + y.x), 0.5f * (z.y - y.y), 0.5f * (z.y + y.y), 0.5f * (y.x - z.x)};
    *(f4*)(X1 + (size_t)k * 2048 + b * 8 + 2 * p) = o;
  }
  if (tid < 4) {
    f2 z = Lp[256 + 32];
    *(f4*)(X1 + (size_t)256 * 2048 + b * 8 + 2 * p) = (f4){z.x, 0.f, z.y, 0.f};
  }
  STAMP(6);
  STAMP_FLUSH(0);
}

__global__ __launch_bounds__(256) void k2_col(const f2* __restrict__ X1, f2* __restrict__ X2,
                                              const unsigned* __restrict__ Wt) {
  __shared__ __attribute__((aligned(16))) f2 lds[8 * 288];
  const int c = blockIdx.x < 256 ? (blockIdx.x & 7) * 32 + (blockIdx.x >> 3) : 256;
  const int t = threadIdx.x;
  const f2* col = X1 + (size_t)c * 2048;
  STAMP_DECL; STAMP(0);
  f2 v[8];
#pragma unroll
  for (int m = 0; m < 8; ++m) v[m] = col[t + 256 * m];
  u4 w0 = *(const u4*)(Wt + (size_t)c * 2048 + t * 8);
  u4 w1 = *(const u4*)(Wt + (size_t)c * 2048 + t * 8 + 4);
  f2 t1[8], t2[8], t3[8];
  make_tw(t1, t, 2.0f / 2048.0f);
  make_tw(t2, t & 31, 2.0f / 256.0f);
  make_tw(t3, t & 3, 2.0f / 32.0f);
  STAMP(1);
  const int g = t >> 5, lo5 = t & 31;
  f2* R = lds + g * 288;
  dft8<false>(v);
#pragma unroll
  for (int k = 1; k < 8; ++k) v[k] = cmul(v[k], t1[k]);
#pragma unroll
  for (int k = 0; k < 8; ++k) lds[k * 288 + t] = v[k];
  STAMP(2);
  __syncthreads();
#pragma unroll
  for (int n = 0; n < 8; ++n) v[n] = R[n * 32 + lo5];
  WAVE_BAR();
  dft8<false>(v);
#pragma unroll
  for (int k = 1; k < 8; ++k) v[k] = cmul(v[k], t2[k]);
#pragma unroll
  for (int k = 0; k < 8; ++k) R[k * 36 + lo5] = v[k];
  WAVE_BAR();
  const int m3 = (t >> 2) & 7, q3 = t & 3;
#pragma unroll
  for (int n = 0; n < 8; ++n) v[n] = R[m3 * 36 + 4 * n + q3];
  WAVE_BAR();
  dft8<false>(v);
#pragma unroll
  for (int k = 1; k < 8; ++k) v[k] = cmul(v[k], t3[k]);
#pragma unroll
  for (int k = 0; k < 8; ++k) R[m3 * 34 + 4 * k + q3] = v[k];
  WAVE_BAR();
#pragma unroll
  for (int e = 0; e < 8; ++e) v[e] = R[m3 * 34 + 8 * q3 + e];
  WAVE_BAR();
  dft4<false>(v[0], v[1], v[2], v[3]);
  dft4<false>(v[4], v[5], v[6], v[7]);
  STAMP(3);
  {
    const float sc = 1.0f / 1048576.0f;
#pragma unroll
    for (int e = 0; e < 4; ++e) {
      v[e] = cmul(v[e], (f2){bf2f(w0[e] & 0xffffu) * sc, bf2f_hi(w0[e]) * sc});
      v[e + 4] = cmul(v[e + 4], (f2){bf2f(w1[e] & 0xffffu) * sc, bf2f_hi(w1[e]) * sc});
    }
  }
  STAMP(4);
  dft4<true>(v[0], v[1], v[2], v[3]);
  dft4<true>(v[4], v[5], v[6], v[7]);
#pragma unroll
  for (int e = 0; e < 8; ++e) R[m3 * 34 + 8 * q3 + e] = v[e];
  WAVE_BAR();
#pragma unroll
  for (int k = 0; k < 8; ++k) v[k] = R[m3 * 34 + 4 * k + q3];
  WAVE_BAR();
#pragma unroll
  for (int k = 1; k < 8; ++k) v[k] = cmulc(v[k], t3[k]);
  dft8<true>(v);
#pragma unroll
  for (int n = 0; n < 8; ++n) R[m3 * 36 + 4 * n + q3] = v[n];
  WAVE_BAR();
#pragma unroll
  for (int k = 0; k < 8; ++k) v[k] = R[k * 36 + lo5];
  WAVE_BAR();
#pragma unroll
  for (int k = 1; k < 8; ++k) v[k] = cmulc(v[k], t2[k]);
  dft8<true>(v);
#pragma unroll
  for (int n = 0; n < 8; ++n) R[n * 32 + lo5] = v[n];
  __syncthreads();
#pragma unroll
  for (int k = 0; k < 8; ++k) v[k] = lds[k * 288 + t];
#pragma unroll
  for (int k = 1; k < 8; ++k) v[k] = cmulc(v[k], t1[k]);
  dft8<true>(v);
  STAMP(5);
  f2* ocol = X2 + (size_t)c * 2048;
#pragma unroll
  for (int m = 0; m < 8; ++m) ocol[t + 256 * m] = v[m];
  STAMP(6);
  STAMP_FLUSH(1);
}

__global__ __launch_bounds__(256) void k3_ifft(const f2* __restrict__ X2, const us* __restrict__ x,
                                               us* __restrict__ h, us* __restrict__ ln2,
                                               const us* __restrict__ wg, const us* __restrict__ wu) {
  __shared__ __attribute__((aligned(16))) f2 lds[4 * WSTR];
  const int tid = threadIdx.x;
  if (blockIdx.x >= 256) {
    const int j = (blockIdx.x - 256) >> 3;
    const char* pg = (const char*)wg + j * 88064 + tid * 16;
    const char* pu = (const char*)wu + j * 88064 + tid * 16;
    u4 acc = {0, 0, 0, 0};
#pragma unroll
    for (int i = 0; i < 21; ++i) { acc ^= *(const u4*)(pg + i * 4096); acc ^= *(const u4*)(pu + i * 4096); }
    if (tid < 128) { acc ^= *(const u4*)(pg + 21 * 4096); acc ^= *(const u4*)(pu + 21 * 4096); }
    asm volatile("" ::"v"(acc));
    return;
  }
  const int b = (blockIdx.x & 7) * 32 + (blockIdx.x >> 3);
  const int w = tid >> 6, l = tid & 63;
  const int p = tid & 3;
  STAMP_DECL; STAMP(0);
  f4 o[4];
#pragma unroll
  for (int i = 0; i < 4; ++i) {
    int k = (tid >> 2) + 64 * i;
    o[i] = *(const f4*)(X2 + (size_t)k * 2048 + b * 8 + 2 * p);
  }
  f4 ony = *(const f4*)(X2 + (size_t)256 * 2048 + b * 8 + 2 * p);
  const int row = b * 8 + w * 2;
  u4 ra = *(const u4*)(x + (size_t)row * 512 + l * 8);
  u4 rb = *(const u4*)(x + (size_t)(row + 1) * 512 + l * 8);
  f2 t1[8], t2[8];
  make_tw(t1, l, 2.0f / 512.0f);
  make_tw(t2, l & 7, 2.0f / 64.0f);
  STAMP(1); STAMPW(2);
  f2* Lp = lds + p * WSTR;
#pragma unroll
  for (int i = 0; i < 4; ++i) {
    int k = (tid >> 2) + 64 * i;
    int kk = (512 - k) & 511;
    f4 z = o[i];
    if (k == 0) {
      Lp[0] = (f2){z.x, z.z};
      Lp[256 + 32] = (f2){ony.x, ony.z};
    } else {
      Lp[k + (k >> 3)] = (f2){z.x - z.w, z.y + z.z};
      Lp[kk + (kk >> 3)] = (f2){z.x + z.w, z.z - z.y};
    }
  }
  __syncthreads();
  STAMP(3);
  f2* L = lds + w * WSTR;
  const int hi = l >> 3, lo = l & 7;
  f2 v[8];
#pragma unroll
  for (int k = 0; k < 8; ++k) v[k] = L[hi + 9 * lo + 72 * k];
  WAVE_BAR();
  dft8<true>(v);
#pragma unroll
  for (int n = 1; n < 8; ++n) v[n] = cmulc(v[n], t2[n]);
#pragma unroll
  for (int n = 0; n < 8; ++n) L[hi * 72 + lo * 9 + n] = v[n];
  WAVE_BAR();
#pragma unroll
  for (int k = 0; k < 8; ++k) v[k] = L[hi * 72 + k * 9 + lo];
  WAVE_BAR();
  dft8<true>(v);
#pragma unroll
  for (int n = 0; n < 8; ++n) L[hi * 72 + n * 8 + lo] = v[n];
  WAVE_BAR();
#pragma unroll
  for (int k = 0; k < 8; ++k) v[k] = L[k * 72 + l];
  WAVE_BAR();
#pragma unroll
  for (int k = 1; k < 8; ++k) v[k] = cmulc(v[k], t1[k]);
  dft8<true>(v);
  STAMP(4);
  unsigned* lp = (unsigned*)L;
#pragma unroll
  for (int m = 0; m < 8; ++m) lp[l + 64 * m] = pack2(v[m].x, v[m].y);
  WAVE_BAR();
  u4 y0 = *(const u4*)(lp + l * 8), y1 = *(const u4*)(lp + l * 8 + 4);
  float a[8], c[8];
#pragma unroll
  for (int j = 0; j < 4; ++j) {
    unsigned ya = y0[j], yb = y1[j];
    float xa0 = (j & 1) ? bf2f_hi(ra[j >> 1]) : bf2f(ra[j >> 1] & 0xffffu);
    float xb0 = (j & 1) ? bf2f_hi(rb[j >> 1]) : bf2f(rb[j >> 1] & 0xffffu);
    float xa1 = (j & 1) ? bf2f_hi(ra[2 + (j >> 1)]) : bf2f(ra[2 + (j >> 1)] & 0xffffu);
    float xb1 = (j & 1) ? bf2f_hi(rb[2 + (j >> 1)]) : bf2f(rb[2 + (j >> 1)] & 0xffffu);
    a[j] = rbf(xa0 + bf2f(ya & 0xffffu));
    c[j] = rbf(xb0 + bf2f_hi(ya));
    a[j + 4] = rbf(xa1 + bf2f(yb & 0xffffu));
    c[j + 4] = rbf(xb1 + bf2f_hi(yb));
  }
  u4 ha, hb;
#pragma unroll
  for (int j = 0; j < 4; ++j) {
    ha[j] = (__float_as_uint(a[2 * j]) >> 16) | (__float_as_uint(a[2 * j + 1]) & 0xffff0000u);
    hb[j] = (__float_as_uint(c[2 * j]) >> 16) | (__float_as_uint(c[2 * j + 1]) & 0xffff0000u);
  }
  *(u4*)(h + (size_t)row * 512 + l * 8) = ha;
  *(u4*)(h + (size_t)(row + 1) * 512 + l * 8) = hb;
  ln_pair(a, c);
#pragma unroll
  for (int j = 0; j < 4; ++j) {
    ha[j] = (__float_as_uint(a[2 * j]) >> 16) | (__float_as_uint(a[2 * j + 1]) & 0xffff0000u);
    hb[j] = (__float_as_uint(c[2 * j]) >> 16) | (__float_as_uint(c[2 * j + 1]) & 0xffff0000u);
  }
  {
    int mt = row >> 7, rbk = (row >> 4) & 7, kb = l >> 2, ch = l & 3;
    size_t base = (((size_t)mt * 16 + kb) * 8 + rbk) * 64 + 16 * ch;
    *(u4*)(ln2 + (base + (row & 15)) * 8) = ha;
    *(u4*)(ln2 + (base + ((row + 1) & 15)) * 8) = hb;
  }
  STAMP(5);
  STAMP_FLUSH(2);
}

#define GLDS(gp, lp) __builtin_amdgcn_global_load_lds((const unsigned*)(gp), (unsigned*)(lp), 16, 0, 0)
#define WAIT_V(n) asm volatile("s_waitcnt vmcnt(" #n ")" ::: "memory")
#define WAIT_L0() asm volatile("s_waitcnt lgkmcnt(0)" ::: "memory")
#define RAW_BAR() do { asm volatile("s_waitcnt lgkmcnt(0)" ::: "memory"); __builtin_amdgcn_s_barrier(); } while (0)
typedef short s4v __attribute__((ext_vector_type(4)));
#define LDS_TR(dst, addr, off) asm volatile("ds_read_b64_tr_b16 %0, %1 offset:%2" : "=v"(dst) : "v"(addr), "i"(off))
#define LDS_B128(dst, addr, off) asm volatile("ds_read_b128 %0, %1 offset:%2" : "=v"(dst) : "v"(addr), "i"(off))
#define WAIT_L(n) asm volatile("s_waitcnt lgkmcnt(" #n ")" ::: "memory")
#define CAT8(a, b) ((bf8){a[0], a[1], a[2], a[3], b[0], b[1], b[2], b[3]})
#define SCHED0() __builtin_amdgcn_sched_barrier(0)

__device__ __forceinline__ float gelu_t(float x) {
  float y2 = -2.3022082007793f * (x + 0.044715f * x * x * x);
  return x * __builtin_amdgcn_rcpf(1.0f + __builtin_amdgcn_exp2f(y2));
}

__global__ __launch_bounds__(512) void k4_gemm1(const us* __restrict__ ln2, const us* __restrict__ wg,
                                                const us* __restrict__ wu, const us* __restrict__ wd,
                                                us* __restrict__ act) {
  __shared__ __attribute__((aligned(16))) char sm[3 * 40960 + 8192];
  const int tid = threadIdx.x, wid = tid >> 6, lane = tid & 63;
  const int xcd = blockIdx.x & 7, idx = blockIdx.x >> 3;
  const int mt = 2 * xcd + (idx >> 4), ft = idx & 15;
  const int wm = wid >> 1, wn = wid & 1;
  STAMP_DECL; STAMP(0);
  const char* gA = (const char*)ln2 + (size_t)mt * 131072 + tid * 16;
  const char* gB[3];
#pragma unroll
  for (int i = 0; i < 3; ++i) {
    int q = i * 512 + tid;
    int mat = q >= 768 ? 1 : 0;
    int qq = q - mat * 768;
    int r = qq / 12, cp = qq - r * 12;
    int cch = cp ^ (((r >> 3) & 1) << 1);
    int col0 = 88 * ft + 8 * cch;
    col0 = col0 > 1368 ? 1368 : col0;
    gB[i] = (const char*)((mat ? wu : wg) + (size_t)r * 1376 + col0);
  }
  auto stage = [&](int s, int buf) {
    char* dst = sm + buf * 40960 + wid * 1024;
    GLDS(gA + s * 16384, dst);
    GLDS(gA + s * 16384 + 8192, dst + 8192);
    GLDS(gB[0] + (size_t)s * 64 * 2752, dst + 16384);
    GLDS(gB[1] + (size_t)s * 64 * 2752, dst + 16384 + 8192);
    GLDS(gB[2] + (size_t)s * 64 * 2752, dst + 16384 + 16384);
  };
  f4 ag[2][3], au[2][3];
#pragma unroll
  for (int i = 0; i < 2; ++i)
#pragma unroll
    for (int f = 0; f < 3; ++f) { ag[i][f] = (f4){0, 0, 0, 0}; au[i][f] = (f4){0, 0, 0, 0}; }
  stage(0, 0);
  stage(1, 1);
  {
    const char* pd = (const char*)wd + (size_t)idx * 44032 + tid * 16;
    char* dump = sm + 3 * 40960 + wid * 1024;
#pragma unroll
    for (int i = 0; i < 5; ++i) GLDS(pd + i * 8192, dump);
    GLDS(pd + (tid < 192 ? 5 * 8192 : 0), dump);
  }
  const int g = lane >> 4, q4 = (lane & 15) >> 2, p4 = lane & 3;
  const unsigned smb = (unsigned)(size_t)sm;
  const unsigned tb = smb + 16384 + (8 * g + q4) * 192 + 8 * (p4 & 1) + 16 * (p4 >> 1) + 96 * wn;
  const int sgn = 32 * (g & 1);
  const unsigned tb0 = tb + (wn ? -sgn : sgn), tb1 = tb + (wn ? sgn : -sgn);
  const unsigned ab = smb + lane * 16 + wm * 2048;
#pragma unroll
  for (int s = 0; s < 8; ++s) {
    if (s < 2) WAIT_V(11); else if (s < 7) WAIT_V(5); else WAIT_V(0);
    RAW_BAR();
    STAMP(1 + s);
    if (s + 2 < 8) stage(s + 2, (s + 2) % 3);
    const unsigned bo = (s % 3) * 40960;
    const unsigned a_ = ab + bo, t0_ = tb0 + bo, t1_ = tb1 + bo;
    bf8 xb[2][2];
    s4v wl[2][2][3][2];
#pragma unroll
    for (int kk = 0; kk < 2; ++kk) {
      LDS_B128(xb[kk][0], a_, kk * 8192);
      LDS_B128(xb[kk][1], a_, kk * 8192 + 1024);
#pragma unroll
      for (int mat = 0; mat < 2; ++mat) {
        LDS_TR(wl[kk][mat][0][0], t0_, kk * 6144 + mat * 12288 + 0);
        LDS_TR(wl[kk][mat][0][1], t0_, kk * 6144 + mat * 12288 + 0 + 768);
        LDS_TR(wl[kk][mat][1][0], t1_, kk * 6144 + mat * 12288 + 32);
        LDS_TR(wl[kk][mat][1][1], t1_, kk * 6144 + mat * 12288 + 32 + 768);
        LDS_TR(wl[kk][mat][2][0], t0_, kk * 6144 + mat * 12288 + 64);
        LDS_TR(wl[kk][mat][2][1], t0_, kk * 6144 + mat * 12288 + 64 + 768);
      }
    }
#pragma unroll
    for (int kk = 0; kk < 2; ++kk) {
      if (kk == 0) WAIT_L(14); else WAIT_L(0);
      SCHED0();
#pragma unroll
      for (int f = 0; f < 3; ++f) {
        bf8 wgf = CAT8(wl[kk][0][f][0], wl[kk][0][f][1]);
        bf8 wuf = CAT8(wl[kk][1][f][0], wl[kk][1][f][1]);
#pragma unroll
        for (int i = 0; i < 2; ++i) {
          ag[i][f] = __builtin_amdgcn_mfma_f32_16x16x32_bf16(wgf, xb[kk][i], ag[i][f], 0, 0, 0);
          au[i][f] = __builtin_amdgcn_mfma_f32_16x16x32_bf16(wuf, xb[kk][i], au[i][f], 0, 0, 0);
        }
      }
      SCHED0();
    }
  }
  STAMP(9);
#pragma unroll
  for (int i = 0; i < 2; ++i) {
    int row = mt * 128 + wm * 32 + i * 16 + (lane & 15);
    int mt2 = row >> 6, rbl = (row >> 4) & 3;
#pragma unroll
    for (int f = 0; f < 3; ++f) {
      int c = wn * 48 + f * 16 + 4 * (lane >> 4);
      int k = 88 * ft + c;
      float r[4];
#pragma unroll
      for (int j = 0; j < 4; ++j) {
        float gg = rbf(ag[i][f][j]), uu = rbf(au[i][f][j]);
        r[j] = rbf(gelu_t(gg)) * uu;
      }
      u2 o = (u2){pack2(r[0], r[1]), pack2(r[2], r[3])};
      size_t off = ((((size_t)mt2 * 43 + (k >> 5)) * 4 + rbl) * 64 + (lane & 15) + 16 * ((k & 31) >> 3)) * 8 + (k & 7);
      if (c < 88 && k < 1376) *(u2*)(act + off) = o;
    }
  }
  STAMP(10);
  STAMP_FLUSH(3);
}

__global__ __launch_bounds__(512) void k5_gemm2(const us* __restrict__ act, const us* __restrict__ wd,
                                                const us* __restrict__ h, us* __restrict__ out) {
  __shared__ __attribute__((aligned(16))) char sm[131072];
  const int tid = threadIdx.x, wid = tid >> 6, lane = tid & 63;
  const int xcd = blockIdx.x & 7, idx = blockIdx.x >> 3;
  const int mt2 = xcd * 4 + (idx >> 3), nt = idx & 7;
  STAMP_DECL; STAMP(0);
  char* ring = sm + wid * 16384;
  const char* gA = (const char*)act + ((size_t)mt2 * 43 * 4) * 1024 + lane * 16;
  const char* gW[4];
#pragma unroll
  for (int i = 0; i < 4; ++i) {
    int rho = 8 * i + (lane >> 3);
    int sw = ((rho >> 1) & 1) | (((rho >> 3) & 1) << 1);
    int ch = (lane & 7) ^ (sw << 1);
    gW[i] = (const char*)(wd + (size_t)rho * 512 + nt * 64 + ch * 8);
  }
  auto stage = [&](int kb, int buf) {
    char* dst = ring + buf * 8192;
#pragma unroll
    for (int i = 0; i < 4; ++i) GLDS(gA + (size_t)kb * 4096 + i * 1024, dst + i * 1024);
#pragma unroll
    for (int i = 0; i < 4; ++i) GLDS(gW[i] + (size_t)kb * 32 * 1024, dst + 4096 + i * 1024);
  };
  f4 acc[4][4];
#pragma unroll
  for (int i = 0; i < 4; ++i)
#pragma unroll
    for (int j = 0; j < 4; ++j) acc[i][j] = (f4){0, 0, 0, 0};
  const int g = lane >> 4, q4 = (lane & 15) >> 2, p4 = lane & 3;
  const int sw = ((q4 >> 1) & 1) | ((g & 1) << 1);
  const unsigned ringb = (unsigned)(size_t)sm + wid * 16384;
  const unsigned trb = ringb + 4096 + (8 * g + q4) * 128 + 8 * (p4 & 1) + 16 * (p4 >> 1);
  const unsigned tr0 = trb + 32 * (0 ^ sw), tr1 = trb + 32 * (1 ^ sw), tr2 = trb + 32 * (2 ^ sw), tr3 = trb + 32 * (3 ^ sw);
  const unsigned ab = ringb + lane * 16;
  const int nst = wid < 3 ? 6 : 5;
  stage(wid, 0);
  stage(wid + 8, 1);
#pragma unroll
  for (int i = 0; i < 6; ++i) {
    if (i < nst) {
      if (i + 1 < nst) WAIT_V(8); else WAIT_V(0);
      bf8 xa[4];
      s4v wl[4][2];
      LDS_B128(xa[0], ab, (i & 1) * 8192 + 0);
      LDS_B128(xa[1], ab, (i & 1) * 8192 + 1024);
      LDS_B128(xa[2], ab, (i & 1) * 8192 + 2048);
      LDS_B128(xa[3], ab, (i & 1) * 8192 + 3072);
      LDS_TR(wl[0][0], tr0, (i & 1) * 8192); LDS_TR(wl[0][1], tr0, (i & 1) * 8192 + 512);
      LDS_TR(wl[1][0], tr1, (i & 1) * 8192); LDS_TR(wl[1][1], tr1, (i & 1) * 8192 + 512);
      LDS_TR(wl[2][0], tr2, (i & 1) * 8192); LDS_TR(wl[2][1], tr2, (i & 1) * 8192 + 512);
      LDS_TR(wl[3][0], tr3, (i & 1) * 8192); LDS_TR(wl[3][1], tr3, (i & 1) * 8192 + 512);
      WAIT_L(0);
      SCHED0();
      if (i + 2 < nst) stage(wid + 8 * (i + 2), i & 1);
#pragma unroll
      for (int nb = 0; nb < 4; ++nb) {
        bf8 wb = CAT8(wl[nb][0], wl[nb][1]);
#pragma unroll
        for (int rb = 0; rb < 4; ++rb)
          acc[nb][rb] = __builtin_amdgcn_mfma_f32_16x16x32_bf16(wb, xa[rb], acc[nb][rb], 0, 0, 0);
      }
      SCHED0();
    }
  }
  STAMP(1);
  __syncthreads();
  f4* red = (f4*)sm;
#pragma unroll
  for (int nb = 0; nb < 4; ++nb)
#pragma unroll
    for (int rb = 0; rb < 4; ++rb) red[(wid * 16 + nb * 4 + rb) * 64 + lane] = acc[nb][rb];
  __syncthreads();
  STAMP(2);
  const int rb = wid >> 1;
#pragma unroll
  for (int q = 0; q < 2; ++q) {
    int nb = 2 * (wid & 1) + q;
    f4 s = red[(nb * 4 + rb) * 64 + lane];
#pragma unroll
    for (int src = 1; src < 8; ++src) s += red[(src * 16 + nb * 4 + rb) * 64 + lane];
    int token = mt2 * 64 + rb * 16 + (lane & 15);
    int col = nt * 64 + nb * 16 + 4 * (lane >> 4);
    u2 hv = *(const u2*)(h + (size_t)token * 512 + col);
    float o0 = bf2f(hv[0] & 0xffffu) + rbf(s[0]);
    float o1 = bf2f_hi(hv[0]) + rbf(s[1]);
    float o2 = bf2f(hv[1] & 0xffffu) + rbf(s[2]);
    float o3 = bf2f_hi(hv[1]) + rbf(s[3]);
    *(u2*)(out + (size_t)token * 512 + col) = (u2){pack2(o0, o1), pack2(o2, o3)};
  }
  STAMP(3);
  STAMP_FLUSH(4);
}

extern "C" void kernel_launch(void* const* d_in, const int* in_sizes, int n_in, void* d_out, int out_size,
                              void* d_ws, size_t ws_size, hipStream_t stream) {
  const us* x = (const us*)d_in[0];
  const us* wr = (const us*)d_in[1];
  const us* wi = (const us*)d_in[2];
  const us* wg = (const us*)d_in[4];
  const us* wu = (const us*)d_in[5];
  const us* wd = (const us*)d_in[6];
  char* ws = (char*)d_ws;
  f2* X1 = (f2*)(ws);
  f2* X2 = (f2*)(ws + (5u << 20));
  us* h = (us*)(ws + (10u << 20));
  us* ln2 = (us*)(ws + (12u << 20));
  us* act = (us*)(ws + (14u << 20));
  unsigned* Wt = (unsigned*)(ws + (20u << 20));
  k1_lnfft<<<512, 256, 0, stream>>>(x, X1, wr, wi, Wt);
  k2_col<<<257, 256, 0, stream>>>(X1, X2, Wt);
  k3_ifft<<<256 + 128, 256, 0, stream>>>(X2, x, h, ln2, wg, wu);
  k4_gemm1<<<256, 512, 0, stream>>>(ln2, wg, wu, wd, act);
  k5_gemm2<<<256, 512, 0, stream>>>(act, wd, h, (us*)d_out);
}
```

```cpp
#include <hip/hip_runtime.h>
#include <hip/hip_bf16.h>
#include <stdint.h>

typedef float f2 __attribute__((ext_vector_type(2)));
typedef float f4 __attribute__((ext_vector_type(4)));
typedef unsigned u4 __attribute__((ext_vector_type(4)));
typedef unsigned u2 __attribute__((ext_vector_type(2)));
typedef short bf8 __attribute__((ext_vector_type(8)));
typedef unsigned short us;

#define WAVE_BAR() __builtin_amdgcn_wave_barrier()
#define STAMP_DECL
#define STAMP(i)
#define STAMPW(i)
#define STAMP_FLUSH(kid)

__device__ __forceinline__ float bf2f(unsigned h) { return __uint_as_float(h << 16); }
__device__ __forceinline__ float bf2f_hi(unsigned h) { return __uint_as_float(h & 0xffff0000u); }
__device__ __forceinline__ unsigned f2bf(float f) {
  __bf16 b = (__bf16)f;
  return (unsigned)__builtin_bit_cast(unsigned short, b);
}
__device__ __forceinline__ float rbf(float f) { return bf2f(f2bf(f)); }
__device__ __forceinline__ unsigned pack2(float lo, float hi) { return f2bf(lo) | (f2bf(hi) << 16); }

__device__ __forceinline__ f2 cmul(f2 a, f2 b) { return (f2){a.x * b.x - a.y * b.y, a.x * b.y + a.y * b.x}; }
__device__ __forceinline__ f2 cmulc(f2 a, f2 b) { return (f2){a.x * b.x + a.y * b.y, a.y * b.x - a.x * b.y}; }
template <bool INV> __device__ __forceinline__ f2 rot(f2 a) { return INV ? (f2){-a.y, a.x} : (f2){a.y, -a.x}; }

template <bool INV> __device__ __forceinline__ void dft8(f2 (&v)[8]) {
  const float r = 0.70710678118654752f;
  f2 a0 = v[0] + v[4], a1 = v[0] - v[4], a2 = v[2] + v[6], a3 = rot<INV>(v[2] - v[6]);
  f2 a4 = v[1] + v[5], a5 = v[1] - v[5], a6 = v[3] + v[7], a7 = rot<INV>(v[3] - v[7]);
  f2 e0 = a0 + a2, e2 = a0 - a2, e1 = a1 + a3, e3 = a1 - a3;
  f2 o0 = a4 + a6, o2 = a4 - a6, o1 = a5 + a7, o3 = a5 - a7;
  o1 = (o1 + rot<INV>(o1)) * r;
  o2 = rot<INV>(o2);
  o3 = (rot<INV>(o3) - o3) * r;
  v[0] = e0 + o0; v[4] = e0 - o0;
  v[1] = e1 + o1; v[5] = e1 - o1;
  v[2] = e2 + o2; v[6] = e2 - o2;
  v[3] = e3 + o3; v[7] = e3 - o3;
}
template <bool INV> __device__ __forceinline__ void dft4(f2& x0, f2& x1, f2& x2, f2& x3) {
  f2 a0 = x0 + x2, a1 = x0 - x2, a2 = x1 + x3, a3 = rot<INV>(x1 - x3);
  x0 = a0 + a2; x2 = a0 - a2; x1 = a1 + a3; x3 = a1 - a3;
}
__device__ __forceinline__ void make_tw(f2 (&t)[8], int num, float c2) {
  float s, c;
  sincospif(-(float)num * c2, &s, &c);
  t[1] = (f2){c, s};
  t[2] = cmul(t[1], t[1]);
  t[3] = cmul(t[2], t[1]);
  t[4] = cmul(t[2], t[2]);
  t[5] = cmul(t[4], t[1]);
  t[6] = cmul(t[3], t[3]);
  t[7] = cmul(t[4], t[3]);
}

template <int ctrl, int rm> __device__ __forceinline__ float dpp_add(float v) {
  int t = __builtin_amdgcn_update_dpp(0, __float_as_int(v), ctrl, rm, 0xf, false);
  return v + __int_as_float(t);
}
__device__ __forceinline__ float wave_sum(float v) {
  v = dpp_add<0x111, 0xf>(v);
  v = dpp_add<0x112, 0xf>(v);
  v = dpp_add<0x114, 0xf>(v);
  v = dpp_add<0x118, 0xf>(v);
  v = dpp_add<0x142, 0xa>(v);
  v = dpp_add<0x143, 0xc>(v);
  return __int_as_float(__builtin_amdgcn_readlane(__float_as_int(v), 63));
}

__device__ __forceinline__ void ln_pair(float (&a)[8], float (&b)[8]) {
  float sa = 0.f, sb = 0.f;
#pragma unroll
  for (int j = 0; j < 8; ++j) { sa += a[j]; sb += b[j]; }
  sa = wave_sum(sa); sb = wave_sum(sb);
  float ma = sa * (1.0f / 512.0f), mb = sb * (1.0f / 512.0f);
  float va = 0.f, vb = 0.f;
#pragma unroll
  for (int j = 0; j < 8; ++j) { float d = a[j] - ma; va += d * d; float e = b[j] - mb; vb += e * e; }
  va = wave_sum(va) * (1.0f / 512.0f); vb = wave_sum(vb) * (1.0f / 512.0f);
  float mra = rbf(ma), mrb = rbf(mb);
  float da = rbf(sqrtf(rbf(rbf(va) + 1e-5f))), db = rbf(sqrtf(rbf(rbf(vb) + 1e-5f)));
#pragma unroll
  for (int j = 0; j < 8; ++j) { a[j] = rbf(rbf(a[j] - mra) / da); b[j] = rbf(rbf(b[j] - mrb) / db); }
}

__device__ __forceinline__ int p_of_q(int q) {
  int T = q >> 3, e = q & 7;
  int k0 = T >> 5, k1 = (T >> 2) & 7, k2 = 2 * (T & 3) + (e >> 2), k3 = e & 3;
  return k0 + 8 * k1 + 64 * k2 + 512 * k3;
}

#define PFD 24
#define PF_DELAY() do { for (int i_ = 0; i_ < PFD; ++i_) __builtin_amdgcn_s_sleep(2); } while (0)
#define WSTR 584

__global__ __launch_bounds__(256) void k1_lnfft(const us* __restrict__ x, f2* __restrict__ X1,
                                                const us* __restrict__ wr, const us* __restrict__ wi,
                                                unsigned* __restrict__ Wt) {
  __shared__ __attribute__((aligned(16))) f2 lds[4 * WSTR];
  const int tid = threadIdx.x;
  if (blockIdx.x >= 256) {
    unsigned* tile = (unsigned*)lds;
    const int xg = blockIdx.x & 7, qi = (blockIdx.x - 256) >> 3;
    const int c = tid & 31;
#pragma unroll
    for (int i = 0; i < 8; ++i) {
      int qq = (tid >> 5) + 8 * i;
      int pp = p_of_q(64 * qi + qq);
      unsigned r_ = wr[pp * 257 + 32 * xg + c], i_ = wi[pp * 257 + 32 * xg + c];
      tile[c * 65 + qq] = r_ | (i_ << 16);
    }
    if (xg == 0 && tid < 64) {
      int pp = p_of_q(64 * qi + tid);
      Wt[(size_t)256 * 2048 + 64 * qi + tid] = (unsigned)wr[pp * 257 + 256] | ((unsigned)wi[pp * 257 + 256] << 16);
    }
    __syncthreads();
#pragma unroll
    for (int i = 0; i < 8; ++i) {
      int cc = (tid >> 6) + 4 * i;
      Wt[(size_t)(32 * xg + cc) * 2048 + 64 * qi + (tid & 63)] = tile[cc * 65 + (tid & 63)];
    }
    return;
  }
  const int b = (blockIdx.x & 7) * 32 + (blockIdx.x >> 3);
  const int w = tid >> 6, l = tid & 63;
  STAMP_DECL; STAMP(0);
  const int row = b * 8 + w * 2;
  u4 ra = *(const u4*)(x + (size_t)row * 512 + l * 8);
  u4 rb = *(const u4*)(x + (size_t)(row + 1) * 512 + l * 8);
  f2 t1[8], t2[8];
  make_tw(t1, l, 2.0f / 512.0f);
  make_tw(t2, l & 7, 2.0f / 64.0f);
  STAMP(1); STAMPW(2);
  float a[8], c[8];
#pragma unroll
  for (int j = 0; j < 4; ++j) {
    a[2 * j] = bf2f(ra[j] & 0xffffu); a[2 * j + 1] = bf2f_hi(ra[j]);
    c[2 * j] = bf2f(rb[j] & 0xffffu); c[2 * j + 1] = bf2f_hi(rb[j]);
  }
  ln_pair(a, c);
  STAMP(3);
  f2* L = lds + w * WSTR;
  unsigned* lp = (unsigned*)L;
  u4 p0, p1;
#pragma unroll
  for (int j = 0; j < 4; ++j) {
    p0[j] = (__float_as_uint(a[j]) >> 16) | (__float_as_uint(c[j]) & 0xffff0000u);
    p1[j] = (__float_as_uint(a[j + 4]) >> 16) | (__float_as_uint(c[j + 4]) & 0xffff0000u);
  }
  *(u4*)(lp + l * 8) = p0;
  *(u4*)(lp + l * 8 + 4) = p1;
  WAVE_BAR();
  f2 v[8];
#pragma unroll
  for (int m = 0; m < 8; ++m) { unsigned q = lp[l + 64 * m]; v[m] = (f2){bf2f(q & 0xffffu), bf2f_hi(q)}; }
  WAVE_BAR();
  dft8<false>(v);
#pragma unroll
  for (int k = 1; k < 8; ++k) v[k] = cmul(v[k], t1[k]);
#pragma unroll
  for (int k = 0; k < 8; ++k) L[k * 72 + l] = v[k];
  WAVE_BAR();
  const int hi = l >> 3, lo = l & 7;
#pragma unroll
  for (int n = 0; n < 8; ++n) v[n] = L[hi * 72 + n * 8 + lo];
  WAVE_BAR();
  dft8<false>(v);
#pragma unroll
  for (int k = 1; k < 8; ++k) v[k] = cmul(v[k], t2[k]);
#pragma unroll
  for (int k = 0; k < 8; ++k) L[hi * 72 + k * 9 + lo] = v[k];
  WAVE_BAR();
#pragma unroll
  for (int n = 0; n < 8; ++n) v[n] = L[hi * 72 + lo * 9 + n];
  WAVE_BAR();
  dft8<false>(v);
#pragma unroll
  for (int k = 0; k < 8; ++k) L[hi + 9 * lo + 72 * k] = v[k];
  STAMP(4);
  __syncthreads();
  STAMP(5);
  const int p = tid & 3;
  const f2* Lp = lds + p * WSTR;
#pragma unroll
  for (int i = 0; i < 4; ++i) {
    int k = (tid >> 2) + 64 * i;
    int kk = (512 - k) & 511;
    f2 z = Lp[k + (k >> 3)];
    f2 y = Lp[kk + (kk >> 3)];
    f4 o = (f4){0.5f * (z.x + y.x), 0.5f * (z.y - y.y), 0.5f * (z.y + y.y), 0.5f * (y.x - z.x)};
    *(f4*)(X1 + (size_t)k * 2048 + b * 8 + 2 * p) = o;
  }
  if (tid < 4) {
    f2 z = Lp[256 + 32];
    *(f4*)(X1 + (size_t)256 * 2048 + b * 8 + 2 * p) = (f4){z.x, 0.f, z.y, 0.f};
  }
  STAMP(6);
  STAMP_FLUSH(0);
}

__global__ __launch_bounds__(256) void k2_col(const f2* __restrict__ X1, f2* __restrict__ X2,
                                              const unsigned* __restrict__ Wt,
                                              const us* __restrict__ wg, const us* __restrict__ wu) {
  __shared__ __attribute__((aligned(16))) f2 lds[8 * 288];
  if (blockIdx.x >= 264) {
    const char* pg = (const char*)wg + ((blockIdx.x - 264) >> 3) * 88064 + threadIdx.x * 16;
    u4 acc = {0, 0, 0, 0};
    PF_DELAY();
#pragma unroll
    for (int i = 0; i < 21; ++i) acc ^= *(const u4*)(pg + i * 4096);
    if (threadIdx.x < 128) acc ^= *(const u4*)(pg + 21 * 4096);
    asm volatile("" ::"v"(acc));
    return;
  }
  if (blockIdx.x > 256) return;
  const int c = blockIdx.x < 256 ? (blockIdx.x & 7) * 32 + (blockIdx.x >> 3) : 256;
  const int t = threadIdx.x;
  const f2* col = X1 + (size_t)c * 2048;
  STAMP_DECL; STAMP(0);
  f2 v[8];
#pragma unroll
  for (int m = 0; m < 8; ++m) v[m] = col[t + 256 * m];
  u4 w0 = *(const u4*)(Wt + (size_t)c * 2048 + t * 8);
  u4 w1 = *(const u4*)(Wt + (size_t)c * 2048 + t * 8 + 4);
  f2 t1[8], t2[8], t3[8];
  make_tw(t1, t, 2.0f / 2048.0f);
  make_tw(t2, t & 31, 2.0f / 256.0f);
  make_tw(t3, t & 3, 2.0f / 32.0f);
  STAMP(1);
  const int g = t >> 5, lo5 = t & 31;
  f2* R = lds + g * 288;
  dft8<false>(v);
#pragma unroll
  for (int k = 1; k < 8; ++k) v[k] = cmul(v[k], t1[k]);
#pragma unroll
  for (int k = 0; k < 8; ++k) lds[k * 288 + t] = v[k];
  STAMP(2);
  __syncthreads();
#pragma unroll
  for (int n = 0; n < 8; ++n) v[n] = R[n * 32 + lo5];
  WAVE_BAR();
  dft8<false>(v);
#pragma unroll
  for (int k = 1; k < 8; ++k) v[k] = cmul(v[k], t2[k]);
#pragma unroll
  for (int k = 0; k < 8; ++k) R[k * 36 + lo5] = v[k];
  WAVE_BAR();
  const int m3 = (t >> 2) & 7, q3 = t & 3;
#pragma unroll
  for (int n = 0; n < 8; ++n) v[n] = R[m3 * 36 + 4 * n + q3];
  WAVE_BAR();
  dft8<false>(v);
#pragma unroll
  for (int k = 1; k < 8; ++k) v[k] = cmul(v[k], t3[k]);
#pragma unroll
  for (int k = 0; k < 8; ++k) R[m3 * 34 + 4 * k + q3] = v[k];
  WAVE_BAR();
#pragma unroll
  for (int e = 0; e < 8; ++e) v[e] = R[m3 * 34 + 8 * q3 + e];
  WAVE_BAR();
  dft4<false>(v[0], v[1], v[2], v[3]);
  dft4<false>(v[4], v[5], v[6], v[7]);
  STAMP(3);
  {
    const float sc = 1.0f / 1048576.0f;
#pragma unroll
    for (int e = 0; e < 4; ++e) {
      v[e] = cmul(v[e], (f2){bf2f(w0[e] & 0xffffu) * sc, bf2f_hi(w0[e]) * sc});
      v[e + 4] = cmul(v[e + 4], (f2){bf2f(w1[e] & 0xffffu) * sc, bf2f_hi(w1[e]) * sc});
    }
  }
  STAMP(4);
  dft4<true>(v[0], v[1], v[2], v[3]);
  dft4<true>(v[4], v[5], v[6], v[7]);
#pragma unroll
  for (int e = 0; e < 8; ++e) R[m3 * 34 + 8 * q3 + e] = v[e];
  WAVE_BAR();
#pragma unroll
  for (int k = 0; k < 8; ++k) v[k] = R[m3 * 34 + 4 * k + q3];
  WAVE_BAR();
#pragma unroll
  for (int k = 1; k < 8; ++k) v[k] = cmulc(v[k], t3[k]);
  dft8<true>(v);
#pragma unroll
  for (int n = 0; n < 8; ++n) R[m3 * 36 + 4 * n + q3] = v[n];
  WAVE_BAR();
#pragma unroll
  for (int k = 0; k < 8; ++k) v[k] = R[k * 36 + lo5];
  WAVE_BAR();
#pragma unroll
  for (int k = 1; k < 8; ++k) v[k] = cmulc(v[k], t2[k]);
  dft8<true>(v);
#pragma unroll
  for (int n = 0; n < 8; ++n) R[n * 32 + lo5] = v[n];
  __syncthreads();
#pragma unroll
  for (int k = 0; k < 8; ++k) v[k] = lds[k * 288 + t];
#pragma unroll
  for (int k = 1; k < 8; ++k) v[k] = cmulc(v[k], t1[k]);
  dft8<true>(v);
  STAMP(5);
  f2* ocol = X2 + (size_t)c * 2048;
#pragma unroll
  for (int m = 0; m < 8; ++m) ocol[t + 256 * m] = v[m];
  STAMP(6);
  STAMP_FLUSH(1);
}

__global__ __launch_bounds__(256) void k3_ifft(const f2* __restrict__ X2, const us* __restrict__ x,
                                               us* __restrict__ h, us* __restrict__ ln2,
                                               const us* __restrict__ wg, const us* __restrict__ wu) {
  __shared__ __attribute__((aligned(16))) f2 lds[4 * WSTR];
  const int tid = threadIdx.x;
  if (blockIdx.x >= 256) {
    const char* pu = (const char*)wu + ((blockIdx.x - 256) >> 3) * 88064 + threadIdx.x * 16;
    u4 acc = {0, 0, 0, 0};
    PF_DELAY();
#pragma unroll
    for (int i = 0; i < 21; ++i) acc ^= *(const u4*)(pu + i * 4096);
    if (threadIdx.x < 128) acc ^= *(const u4*)(pu + 21 * 4096);
    asm volatile("" ::"v"(acc));
    return;
  }
  const int b = (blockIdx.x & 7) * 32 + (blockIdx.x >> 3);
  const int w = tid >> 6, l = tid & 63;
  const int p = tid & 3;
  STAMP_DECL; STAMP(0);
  f4 o[4];
#pragma unroll
  for (int i = 0; i < 4; ++i) {
    int k = (tid >> 2) + 64 * i;
    o[i] = *(const f4*)(X2 + (size_t)k * 2048 + b * 8 + 2 * p);
  }
  f4 ony = *(const f4*)(X2 + (size_t)256 * 2048 + b * 8 + 2 * p);
  const int row = b * 8 + w * 2;
  u4 ra = *(const u4*)(x + (size_t)row * 512 + l * 8);
  u4 rb = *(const u4*)(x + (size_t)(row + 1) * 512 + l * 8);
  f2 t1[8], t2[8];
  make_tw(t1, l, 2.0f / 512.0f);
  make_tw(t2, l & 7, 2.0f / 64.0f);
  STAMP(1); STAMPW(2);
  f2* Lp = lds + p * WSTR;
#pragma unroll
  for (int i = 0; i < 4; ++i) {
    int k = (tid >> 2) + 64 * i;
    int kk = (512 - k) & 511;
    f4 z = o[i];
    if (k == 0) {
      Lp[0] = (f2){z.x, z.z};
      Lp[256 + 32] = (f2){ony.x, ony.z};
    } else {
      Lp[k + (k >> 3)] = (f2){z.x - z.w, z.y + z.z};
      Lp[kk + (kk >> 3)] = (f2){z.x + z.w, z.z - z.y};
    }
  }
  __syncthreads();
  STAMP(3);
  f2* L = lds + w * WSTR;
  const int hi = l >> 3, lo = l & 7;
  f2 v[8];
#pragma unroll
  for (int k = 0; k < 8; ++k) v[k] = L[hi + 9 * lo + 72 * k];
  WAVE_BAR();
  dft8<true>(v);
#pragma unroll
  for (int n = 1; n < 8; ++n) v[n] = cmulc(v[n], t2[n]);
#pragma unroll
  for (int n = 0; n < 8; ++n) L[hi * 72 + lo * 9 + n] = v[n];
  WAVE_BAR();
#pragma unroll
  for (int k = 0; k < 8; ++k) v[k] = L[hi * 72 + k * 9 + lo];
  WAVE_BAR();
  dft8<true>(v);
#pragma unroll
  for (int n = 0; n < 8; ++n) L[hi * 72 + n * 8 + lo] = v[n];
  WAVE_BAR();
#pragma unroll
  for (int k = 0; k < 8; ++k) v[k] = L[k * 72 + l];
  WAVE_BAR();
#pragma unroll
  for (int k = 1; k < 8; ++k) v[k] = cmulc(v[k], t1[k]);
  dft8<true>(v);
  STAMP(4);
  unsigned* lp = (unsigned*)L;
#pragma unroll
  for (int m = 0; m < 8; ++m) lp[l + 64 * m] = pack2(v[m].x, v[m].y);
  WAVE_BAR();
  u4 y0 = *(const u4*)(lp + l * 8), y1 = *(const u4*)(lp + l * 8 + 4);
  float a[8], c[8];
#pragma unroll
  for (int j = 0; j < 4; ++j) {
    unsigned ya = y0[j], yb = y1[j];
    float xa0 = (j & 1) ? bf2f_hi(ra[j >> 1]) : bf2f(ra[j >> 1] & 0xffffu);
    float xb0 = (j & 1) ? bf2f_hi(rb[j >> 1]) : bf2f(rb[j >> 1] & 0xffffu);
    float xa1 = (j & 1) ? bf2f_hi(ra[2 + (j >> 1)]) : bf2f(ra[2 + (j >> 1)] & 0xffffu);
    float xb1 = (j & 1) ? bf2f_hi(rb[2 + (j >> 1)]) : bf2f(rb[2 + (j >> 1)] & 0xffffu);
    a[j] = rbf(xa0 + bf2f(ya & 0xffffu));
    c[j] = rbf(xb0 + bf2f_hi(ya));
    a[j + 4] = rbf(xa1 + bf2f(yb & 0xffffu));
    c[j + 4] = rbf(xb1 + bf2f_hi(yb));
  }
  u4 ha, hb;
#pragma unroll
  for (int j = 0; j < 4; ++j) {
    ha[j] = (__float_as_uint(a[2 * j]) >> 16) | (__float_as_uint(a[2 * j + 1]) & 0xffff0000u);
    hb[j] = (__float_as_uint(c[2 * j]) >> 16) | (__float_as_uint(c[2 * j + 1]) & 0xffff0000u);
  }
  *(u4*)(h + (size_t)row * 512 + l * 8) = ha;
  *(u4*)(h + (size_t)(row + 1) * 512 + l * 8) = hb;
  ln_pair(a, c);
#pragma unroll
  for (int j = 0; j < 4; ++j) {
    ha[j] = (__float_as_uint(a[2 * j]) >> 16) | (__float_as_uint(a[2 * j + 1]) & 0xffff0000u);
    hb[j] = (__float_as_uint(c[2 * j]) >> 16) | (__float_as_uint(c[2 * j + 1]) & 0xffff0000u);
  }
  {
    int mt = row >> 7, rbk = (row >> 4) & 7, kb = l >> 2, ch = l & 3;
    size_t base = (((size_t)mt * 16 + kb) * 8 + rbk) * 64 + 16 * ch;
    *(u4*)(ln2 + (base + (row & 15)) * 8) = ha;
    *(u4*)(ln2 + (base + ((row + 1) & 15)) * 8) = hb;
  }
  STAMP(5);
  STAMP_FLUSH(2);
}

#define EXP_PD(x) (x)
#define GLDS(gp, lp) __builtin_amdgcn_global_load_lds((const unsigned*)(gp), (unsigned*)(lp), 16, 0, 0)
#define WAIT_V(n) asm volatile("s_waitcnt vmcnt(" #n ")" ::: "memory")
#define WAIT_L0() asm volatile("s_waitcnt lgkmcnt(0)" ::: "memory")
#define RAW_BAR() do { asm volatile("s_waitcnt lgkmcnt(0)" ::: "memory"); __builtin_amdgcn_s_barrier(); } while (0)
typedef short s4v __attribute__((ext_vector_type(4)));
#define LDS_TR(dst, addr, off) asm volatile("ds_read_b64_tr_b16 %0, %1 offset:%2" : "=v"(dst) : "v"(addr), "i"(off))
#define LDS_B128(dst, addr, off) asm volatile("ds_read_b128 %0, %1 offset:%2" : "=v"(dst) : "v"(addr), "i"(off))
#define WAIT_L(n) asm volatile("s_waitcnt lgkmcnt(" #n ")" ::: "memory")
#define CAT8(a, b) ((bf8){a[0], a[1], a[2], a[3], b[0], b[1], b[2], b[3]})
#define SCHED0() __builtin_amdgcn_sched_barrier(0)

__device__ __forceinline__ float gelu_t(float x) {
  float y2 = -2.3022082007793f * (x + 0.044715f * x * x * x);
  return x * __builtin_amdgcn_rcpf(1.0f + __builtin_amdgcn_exp2f(y2));
}

__global__ __launch_bounds__(512) void k4_gemm1(const us* __restrict__ ln2, const us* __restrict__ wg,
                                                const us* __restrict__ wu, const us* __restrict__ wd,
                                                us* __restrict__ act) {
  __shared__ __attribute__((aligned(16))) char sm[3 * 40960 + 8192];
  const int tid = threadIdx.x, wid = tid >> 6, lane = tid & 63;
  const int xcd = blockIdx.x & 7, idx = blockIdx.x >> 3;
  const int mt = 2 * xcd + (idx >> 4), ft = idx & 15;
  const int wm = wid >> 1, wn = wid & 1;
  STAMP_DECL; STAMP(0);
  const char* gA = (const char*)ln2 + (size_t)mt * 131072 + tid * 16;
  const char* gB[3];
#pragma unroll
  for (int i = 0; i < 3; ++i) {
    int q = i * 512 + tid;
    int mat = q >= 768 ? 1 : 0;
    int qq = q - mat * 768;
    int r = qq / 12, cp = qq - r * 12;
    int cch = cp ^ (((r >> 3) & 1) << 1);
    int col0 = 88 * ft + 8 * cch;
    col0 = col0 > 1368 ? 1368 : col0;
    gB[i] = (const char*)((mat ? wu : wg) + (size_t)r * 1376 + col0);
  }
  auto stage = [&](int s, int buf) {
    char* dst = sm + buf * 40960 + wid * 1024;
    GLDS(gA + s * 16384, dst);
    GLDS(gA + s * 16384 + 8192, dst + 8192);
    GLDS(gB[0] + (size_t)s * 64 * 2752, dst + 16384);
    GLDS(gB[1] + (size_t)s * 64 * 2752, dst + 16384 + 8192);
    GLDS(gB[2] + (size_t)s * 64 * 2752, dst + 16384 + 16384);
  };
  f4 ag[2][3], au[2][3];
#pragma unroll
  for (int i = 0; i < 2; ++i)
#pragma unroll
    for (int f = 0; f < 3; ++f) { ag[i][f] = (f4){0, 0, 0, 0}; au[i][f] = (f4){0, 0, 0, 0}; }
  STAMP(12);
  stage(0, 0);
  STAMP(13);
  __builtin_amdgcn_s_barrier();
  stage(1, 1);
  stage(2, 2);
  STAMP(14);
  const int g = lane >> 4, q4 = (lane & 15) >> 2, p4 = lane & 3;
  const unsigned smb = (unsigned)(size_t)sm;
  const unsigned tb = smb + 16384 + (8 * g + q4) * 192 + 8 * (p4 & 1) + 16 * (p4 >> 1) + 96 * wn;
  const int sgn = 32 * (g & 1);
  const unsigned tb0 = tb + (wn ? -sgn : sgn), tb1 = tb + (wn ? sgn : -sgn);
  const unsigned ab = smb + lane * 16 + wm * 2048;
  bf8 xb[2][2][2];
  s4v wl[2][2][2][3][2];
#define RD1(st, set, r) do {                                                                        \
    const unsigned bo = ((st) % 3) * 40960;                                                         \
    constexpr int kk_ = (r) / 14, e_ = (r) % 14;                                                    \
    if constexpr (e_ < 2) { LDS_B128(xb[set][kk_][e_], ab + bo, kk_ * 8192 + e_ * 1024); }          \
    else { constexpr int m_ = (e_ - 2) / 6, f_ = ((e_ - 2) % 6) / 2, t_ = (e_ - 2) % 2;             \
      LDS_TR(wl[set][kk_][m_][f_][t_], ((f_ & 1) ? tb1 : tb0) + bo, kk_ * 6144 + m_ * 12288 + f_ * 32 + t_ * 768); } \
  } while (0)
#define RD4(st, set, r) do { RD1(st, set, r); RD1(st, set, r + 1); RD1(st, set, r + 2); RD1(st, set, r + 3); } while (0)
  WAIT_V(10);
  RAW_BAR();
  STAMP(1);
  RD4(0, 0, 0); RD4(0, 0, 4); RD4(0, 0, 8); RD4(0, 0, 12); RD4(0, 0, 16); RD4(0, 0, 20); RD4(0, 0, 24);
  const char* pd = (const char*)wd + (size_t)idx * 44032 + tid * 16;
  char* dump = sm + 3 * 40960 + wid * 1024;
#pragma unroll
  for (int s = 0; s < 8; ++s) {
    const int cur = s & 1, nx = cur ^ 1;
    if (s < 6) WAIT_V(5); else if (s == 6) WAIT_V(6);
    RAW_BAR();
    SCHED0();
    STAMP(2 + s);
    char* dst = sm + (s % 3) * 40960 + wid * 1024;
#define MM(kk, f, i) do {                                                                                   \
      ag[i][f] = __builtin_amdgcn_mfma_f32_16x16x32_bf16(CAT8(wl[cur][kk][0][f][0], wl[cur][kk][0][f][1]),    \
                                                         xb[cur][kk][i], ag[i][f], 0, 0, 0);                \
      au[i][f] = __builtin_amdgcn_mfma_f32_16x16x32_bf16(CAT8(wl[cur][kk][1][f][0], wl[cur][kk][1][f][1]),    \
                                                         xb[cur][kk][i], au[i][f], 0, 0, 0); } while (0)
#define DMA(j) do {                                                                                  \
      if (s + 3 < 8) {                                                                               \
        if (j == 0) GLDS(gA + (s + 3) * 16384, dst);                                                 \
        if (j == 1) GLDS(gA + (s + 3) * 16384 + 8192, dst + 8192);                                   \
        if (j >= 2 && j < 5) GLDS(gB[j >= 2 ? j - 2 : 0] + (size_t)(s + 3) * 64 * 2752, dst + 16384 + (j - 2) * 8192); \
      } else if (s == 5) {                                                                           \
        if (j < 5) GLDS(pd + j * 8192, dump);                                                        \
        else GLDS(pd + (tid < 192 ? 5 * 8192 : 0), dump);                                            \
      } } while (0)
#define RDN(r) do { if (s + 1 < 8) { RD1(s + 1, nx, r); RD1(s + 1, nx, r + 1); } } while (0)
#define RDN3(r) do { if (s + 1 < 8) { RD1(s + 1, nx, r); RD1(s + 1, nx, r + 1); RD1(s + 1, nx, r + 2); } } while (0)
    MM(0, 0, 0); SCHED0(); DMA(0); RDN(0);   SCHED0();
    MM(0, 0, 1); SCHED0(); RDN3(2);          SCHED0();
    MM(0, 1, 0); SCHED0(); DMA(1); RDN(5);   SCHED0();
    MM(0, 1, 1); SCHED0(); RDN3(7);          SCHED0();
    MM(0, 2, 0); SCHED0(); DMA(2); RDN(10);  SCHED0();
    MM(0, 2, 1); SCHED0(); RDN3(12);         SCHED0();
    MM(1, 0, 0); SCHED0(); DMA(3); RDN(15);  SCHED0();
    MM(1, 0, 1); SCHED0(); RDN3(17);         SCHED0();
    MM(1, 1, 0); SCHED0(); DMA(4); RDN(20);  SCHED0();
    MM(1, 1, 1); SCHED0(); RDN3(22);         SCHED0();
    MM(1, 2, 0); SCHED0(); DMA(5); RDN3(25); SCHED0();
    MM(1, 2, 1); SCHED0();
  }
  STAMP(10);
#pragma unroll
  for (int i = 0; i < 2; ++i) {
    int row = mt * 128 + wm * 32 + i * 16 + (lane & 15);
    int mt2 = row >> 6, rbl = (row >> 4) & 3;
#pragma unroll
    for (int f = 0; f < 3; ++f) {
      int c = wn * 48 + f * 16 + 4 * (lane >> 4);
      int k = 88 * ft + c;
      float r[4];
#pragma unroll
      for (int j = 0; j < 4; ++j) {
        float gg = rbf(ag[i][f][j]), uu = rbf(au[i][f][j]);
        r[j] = rbf(gelu_t(gg)) * uu;
      }
      u2 o = (u2){pack2(r[0], r[1]), pack2(r[2], r[3])};
      size_t off = ((((size_t)mt2 * 43 + (k >> 5)) * 4 + rbl) * 64 + (lane & 15) + 16 * ((k & 31) >> 3)) * 8 + (k & 7);
      if (c < 88 && k < 1376) *(u2*)(act + off) = o;
    }
  }
  WAIT_V(0);
  STAMP(11);
  STAMP_FLUSH(3);
}

__global__ __launch_bounds__(512) void k5_gemm2(const us* __restrict__ act, const us* __restrict__ wd,
                                                const us* __restrict__ h, us* __restrict__ out) {
  __shared__ __attribute__((aligned(16))) char sm[131072];
  const int tid = threadIdx.x, wid = tid >> 6, lane = tid & 63;
  const int xcd = blockIdx.x & 7, idx = blockIdx.x >> 3;
  const int mt2 = xcd * 4 + (idx >> 3), nt = idx & 7;
  STAMP_DECL; STAMP(0);
  char* ring = sm + wid * 16384;
  const char* gA = (const char*)act + ((size_t)mt2 * 43 * 4) * 1024 + lane * 16;
  const char* gW[4];
#pragma unroll
  for (int i = 0; i < 4; ++i) {
    int rho = 8 * i + (lane >> 3);
    int sw = ((rho >> 1) & 1) | (((rho >> 3) & 1) << 1);
    int ch = (lane & 7) ^ (sw << 1);
    gW[i] = (const char*)(wd + (size_t)rho * 512 + nt * 64 + ch * 8);
  }
  auto stage = [&](int kb, int buf) {
    char* dst = ring + buf * 8192;
#pragma unroll
    for (int i = 0; i < 4; ++i) GLDS(gA + (size_t)kb * 4096 + i * 1024, dst + i * 1024);
#pragma unroll
    for (int i = 0; i < 4; ++i) GLDS(gW[i] + (size_t)kb * 32 * 1024, dst + 4096 + i * 1024);
  };
  f4 acc[4][4];
#pragma unroll
  for (int i = 0; i < 4; ++i)
#pragma unroll
    for (int j = 0; j < 4; ++j) acc[i][j] = (f4){0, 0, 0, 0};
  const int g = lane >> 4, q4 = (lane & 15) >> 2, p4 = lane & 3;
  const int sw = ((q4 >> 1) & 1) | ((g & 1) << 1);
  const unsigned ringb = (unsigned)(size_t)sm + wid * 16384;
  const unsigned trb = ringb + 4096 + (8 * g + q4) * 128 + 8 * (p4 & 1) + 16 * (p4 >> 1);
  const unsigned tr0 = trb + 32 * (0 ^ sw), tr1 = trb + 32 * (1 ^ sw), tr2 = trb + 32 * (2 ^ sw), tr3 = trb + 32 * (3 ^ sw);
  const unsigned ab = ringb + lane * 16;
  const int nst = wid < 3 ? 6 : 5;
  stage(wid, 0);
  stage(wid + 8, 1);
#pragma unroll
  for (int i = 0; i < 6; ++i) {
    if (i < nst) {
      if (i + 1 < nst) WAIT_V(8); else WAIT_V(0);
      bf8 xa[4];
      s4v wl[4][2];
      LDS_B128(xa[0], ab, (i & 1) * 8192 + 0);
      LDS_B128(xa[1], ab, (i & 1) * 8192 + 1024);
      LDS_B128(xa[2], ab, (i & 1) * 8192 + 2048);
      LDS_B128(xa[3], ab, (i & 1) * 8192 + 3072);
      LDS_TR(wl[0][0], tr0, (i & 1) * 8192); LDS_TR(wl[0][1], tr0, (i & 1) * 8192 + 512);
      LDS_TR(wl[1][0], tr1, (i & 1) * 8192); LDS_TR(wl[1][1], tr1, (i & 1) * 8192 + 512);
      LDS_TR(wl[2][0], tr2, (i & 1) * 8192); LDS_TR(wl[2][1], tr2, (i & 1) * 8192 + 512);
      LDS_TR(wl[3][0], tr3, (i & 1) * 8192); LDS_TR(wl[3][1], tr3, (i & 1) * 8192 + 512);
      WAIT_L(0);
      SCHED0();
      if (i + 2 < nst) stage(wid + 8 * (i + 2), i & 1);
#pragma unroll
      for (int nb = 0; nb < 4; ++nb) {
        bf8 wb = CAT8(wl[nb][0], wl[nb][1]);
#pragma unroll
        for (int rb = 0; rb < 4; ++rb)
          acc[nb][rb] = __builtin_amdgcn_mfma_f32_16x16x32_bf16(wb, xa[rb], acc[nb][rb], 0, 0, 0);
      }
      SCHED0();
    }
  }
  STAMP(1);
  __syncthreads();
  f4* red = (f4*)sm;
#pragma unroll
  for (int nb = 0; nb < 4; ++nb)
#pragma unroll
    for (int rb = 0; rb < 4; ++rb) red[(wid * 16 + nb * 4 + rb) * 64 + lane] = acc[nb][rb];
  __syncthreads();
  STAMP(2);
  const int rb = wid >> 1;
#pragma unroll
  for (int q = 0; q < 2; ++q) {
    int nb = 2 * (wid & 1) + q;
    f4 s = red[(nb * 4 + rb) * 64 + lane];
#pragma unroll
    for (int src = 1; src < 8; ++src) s += red[(src * 16 + nb * 4 + rb) * 64 + lane];
    int token = mt2 * 64 + rb * 16 + (lane & 15);
    int col = nt * 64 + nb * 16 + 4 * (lane >> 4);
    u2 hv = *(const u2*)(h + (size_t)token * 512 + col);
    float o0 = bf2f(hv[0] & 0xffffu) + rbf(s[0]);
    float o1 = bf2f_hi(hv[0]) + rbf(s[1]);
    float o2 = bf2f(hv[1] & 0xffffu) + rbf(s[2]);
    float o3 = bf2f_hi(hv[1]) + rbf(s[3]);
    *(u2*)(out + (size_t)token * 512 + col) = (u2){pack2(o0, o1), pack2(o2, o3)};
  }
  STAMP(3);
  STAMP_FLUSH(4);
}

extern "C" void kernel_launch(void* const* d_in, const int* in_sizes, int n_in, void* d_out, int out_size,
                              void* d_ws, size_t ws_size, hipStream_t stream) {
  const us* x = (const us*)d_in[0];
  const us* wr = (const us*)d_in[1];
  const us* wi = (const us*)d_in[2];
  const us* wg = (const us*)d_in[4];
  const us* wu = (const us*)d_in[5];
  const us* wd = (const us*)d_in[6];
  char* ws = (char*)d_ws;
  f2* X1 = (f2*)(ws);
  f2* X2 = (f2*)(ws + (5u << 20));
  us* h = (us*)(ws + (10u << 20));
  us* ln2 = (us*)(ws + (12u << 20));
  us* act = (us*)(ws + (14u << 20));
  unsigned* Wt = (unsigned*)(ws + (20u << 20));
  k1_lnfft<<<512, 256, 0, stream>>>(x, X1, wr, wi, Wt);
  k2_col<<<264 + 128, 256, 0, stream>>>(X1, X2, Wt, wg, wu);
  k3_ifft<<<256 + 128, 256, 0, stream>>>(X2, x, h, ln2, wg, wu);
  k4_gemm1<<<256, 512, 0, stream>>>(ln2, wg, wu, wd, act);
  k5_gemm2<<<256, 512, 0, stream>>>(act, wd, h, (us*)d_out);
}
```

```cpp
#include <hip/hip_runtime.h>
#include <hip/hip_bf16.h>
#include <stdint.h>

typedef float f2 __attribute__((ext_vector_type(2)));
typedef float f4 __attribute__((ext_vector_type(4)));
typedef unsigned u4 __attribute__((ext_vector_type(4)));
typedef unsigned u2 __attribute__((ext_vector_type(2)));
typedef short bf8 __attribute__((ext_vector_type(8)));
typedef unsigned short us;

#define WAVE_BAR() __builtin_amdgcn_wave_barrier()
#define ST16(ptr, val) asm volatile("global_store_dwordx4 %0, %1, off sc1\n\ts_nop 1" :: "v"(ptr), "v"(val) : "memory")
#define ST8(ptr, val) asm volatile("global_store_dwordx2 %0, %1, off sc1\n\ts_nop 1" :: "v"(ptr), "v"(val) : "memory")
#define STAMP_DECL
#define STAMP(i)
#define STAMPW(i)
#define STAMP_FLUSH(kid)

__device__ __forceinline__ float bf2f(unsigned h) { return __uint_as_float(h << 16); }
__device__ __forceinline__ float bf2f_hi(unsigned h) { return __uint_as_float(h & 0xffff0000u); }
__device__ __forceinline__ unsigned f2bf(float f) {
  __bf16 b = (__bf16)f;
  return (unsigned)__builtin_bit_cast(unsigned short, b);
}
__device__ __forceinline__ float rbf(float f) { return bf2f(f2bf(f)); }
__device__ __forceinline__ unsigned pack2(float lo, float hi) { return f2bf(lo) | (f2bf(hi) << 16); }

__device__ __forceinline__ f2 cmul(f2 a, f2 b) { return (f2){a.x * b.x - a.y * b.y, a.x * b.y + a.y * b.x}; }
__device__ __forceinline__ f2 cmulc(f2 a, f2 b) { return (f2){a.x * b.x + a.y * b.y, a.y * b.x - a.x * b.y}; }
template <bool INV> __device__ __forceinline__ f2 rot(f2 a) { return INV ? (f2){-a.y, a.x} : (f2){a.y, -a.x}; }

template <bool INV> __device__ __forceinline__ void dft8(f2 (&v)[8]) {
  const float r = 0.70710678118654752f;
  f2 a0 = v[0] + v[4], a1 = v[0] - v[4], a2 = v[2] + v[6], a3 = rot<INV>(v[2] - v[6]);
  f2 a4 = v[1] + v[5], a5 = v[1] - v[5], a6 = v[3] + v[7], a7 = rot<INV>(v[3] - v[7]);
  f2 e0 = a0 + a2, e2 = a0 - a2, e1 = a1 + a3, e3 = a1 - a3;
  f2 o0 = a4 + a6, o2 = a4 - a6, o1 = a5 + a7, o3 = a5 - a7;
  o1 = (o1 + rot<INV>(o1)) * r;
  o2 = rot<INV>(o2);
  o3 = (rot<INV>(o3) - o3) * r;
  v[0] = e0 + o0; v[4] = e0 - o0;
  v[1] = e1 + o1; v[5] = e1 - o1;
  v[2] = e2 + o2; v[6] = e2 - o2;
  v[3] = e3 + o3; v[7] = e3 - o3;
}
template <bool INV> __device__ __forceinline__ void dft4(f2& x0, f2& x1, f2& x2, f2& x3) {
  f2 a0 = x0 + x2, a1 = x0 - x2, a2 = x1 + x3, a3 = rot<INV>(x1 - x3);
  x0 = a0 + a2; x2 = a0 - a2; x1 = a1 + a3; x3 = a1 - a3;
}
__device__ __forceinline__ void make_tw(f2 (&t)[8], int num, float c2) {
  float s, c;
  sincospif(-(float)num * c2, &s, &c);
  t[1] = (f2){c, s};
  t[2] = cmul(t[1], t[1]);
  t[3] = cmul(t[2], t[1]);
  t[4] = cmul(t[2], t[2]);
  t[5] = cmul(t[4], t[1]);
  t[6] = cmul(t[3], t[3]);
  t[7] = cmul(t[4], t[3]);
}

template <int ctrl, int rm> __device__ __forceinline__ float dpp_add(float v) {
  int t = __builtin_amdgcn_update_dpp(0, __float_as_int(v), ctrl, rm, 0xf, false);
  return v + __int_as_float(t);
}
__device__ __forceinline__ float wave_sum(float v) {
  v = dpp_add<0x111, 0xf>(v);
  v = dpp_add<0x112, 0xf>(v);
  v = dpp_add<0x114, 0xf>(v);
  v = dpp_add<0x118, 0xf>(v);
  v = dpp_add<0x142, 0xa>(v);
  v = dpp_add<0x143, 0xc>(v);
  return __int_as_float(__builtin_amdgcn_readlane(__float_as_int(v), 63));
}

__device__ __forceinline__ void ln_pair(float (&a)[8], float (&b)[8]) {
  float sa = 0.f, sb = 0.f;
#pragma unroll
  for (int j = 0; j < 8; ++j) { sa += a[j]; sb += b[j]; }
  sa = wave_sum(sa); sb = wave_sum(sb);
  float ma = sa * (1.0f / 512.0f), mb = sb * (1.0f / 512.0f);
  float va = 0.f, vb = 0.f;
#pragma unroll
  for (int j = 0; j < 8; ++j) { float d = a[j] - ma; va += d * d; float e = b[j] - mb; vb += e * e; }
  va = wave_sum(va) * (1.0f / 512.0f); vb = wave_sum(vb) * (1.0f / 512.0f);
  float mra = rbf(ma), mrb = rbf(mb);
  float da = rbf(sqrtf(rbf(rbf(va) + 1e-5f))), db = rbf(sqrtf(rbf(rbf(vb) + 1e-5f)));
  const float ra_ = 1.0f / da, rb_ = 1.0f / db;
#pragma unroll
  for (int j = 0; j < 8; ++j) {
    float na = rbf(a[j] - mra), nb = rbf(b[j] - mrb);
    float qa = na * ra_, qb = nb * rb_;
    qa = __builtin_fmaf(__builtin_fmaf(-qa, da, na), ra_, qa);
    qb = __builtin_fmaf(__builtin_fmaf(-qb, db, nb), rb_, qb);
    a[j] = rbf(qa); b[j] = rbf(qb);
  }
}

__device__ __forceinline__ int p_of_q(int q) {
  int T = q >> 3, e = q & 7;
  int k0 = T >> 5, k1 = (T >> 2) & 7, k2 = 2 * (T & 3) + (e >> 2), k3 = e & 3;
  return k0 + 8 * k1 + 64 * k2 + 512 * k3;
}

#define PFD 24
#define PF_DELAY() do { for (int i_ = 0; i_ < PFD; ++i_) __builtin_amdgcn_s_sleep(2); } while (0)
#define WSTR 584

__global__ __launch_bounds__(256) void k1_lnfft(const us* __restrict__ x, f2* __restrict__ X1,
                                                const us* __restrict__ wr, const us* __restrict__ wi,
                                                unsigned* __restrict__ Wt) {
  __shared__ __attribute__((aligned(16))) f2 lds[4 * WSTR];
  const int tid = threadIdx.x;
  if (blockIdx.x >= 256) {
    unsigned* tile = (unsigned*)lds;
    const int xg = blockIdx.x & 7, qi = (blockIdx.x - 256) >> 3;
    const int c = tid & 31;
#pragma unroll
    for (int i = 0; i < 8; ++i) {
      int qq = (tid >> 5) + 8 * i;
      int pp = p_of_q(64 * qi + qq);
      unsigned r_ = wr[pp * 257 + 32 * xg + c], i_ = wi[pp * 257 + 32 * xg + c];
      tile[c * 65 + qq] = r_ | (i_ << 16);
    }
    if (xg == 0 && tid < 64) {
      int q = 64 * qi + tid;
      int pp = p_of_q(q), pm = (2048 - pp) & 2047;
      float r0p = bf2f(wr[pp * 257]), i0p = bf2f(wi[pp * 257]);
      float r0m = bf2f(wr[pm * 257]), i0m = bf2f(wi[pm * 257]);
      float r2p = bf2f(wr[pp * 257 + 256]), i2p = bf2f(wi[pp * 257 + 256]);
      float r2m = bf2f(wr[pm * 257 + 256]), i2m = bf2f(wi[pm * 257 + 256]);
      const float sc = 0.25f / 1048576.0f;
      float s0r = r0p + r0m, s0i = i0p - i0m, s2r = r2p + r2m, s2i = i2p - i2m;
      ((f4*)(Wt + (size_t)256 * 2048))[q] = (f4){(s0r + s2r) * sc, (s0i + s2i) * sc, (s0r - s2r) * sc, (s0i - s2i) * sc};
    }
    __syncthreads();
#pragma unroll
    for (int i = 0; i < 8; ++i) {
      int cc = (tid >> 6) + 4 * i;
      Wt[(size_t)(32 * xg + cc) * 2048 + 64 * qi + (tid & 63)] = tile[cc * 65 + (tid & 63)];
    }
    return;
  }
  const int b = (blockIdx.x & 7) * 32 + (blockIdx.x >> 3);
  const int w = tid >> 6, l = tid & 63;
  STAMP_DECL; STAMP(0);
  const int row = b * 8 + w * 2;
  u4 ra = *(const u4*)(x + (size_t)row * 512 + l * 8);
  u4 rb = *(const u4*)(x + (size_t)(row + 1) * 512 + l * 8);
  f2 t1[8], t2[8];
  make_tw(t1, l, 2.0f / 512.0f);
  make_tw(t2, l & 7, 2.0f / 64.0f);
  STAMP(1); STAMPW(2);
  float a[8], c[8];
#pragma unroll
  for (int j = 0; j < 4; ++j) {
    a[2 * j] = bf2f(ra[j] & 0xffffu); a[2 * j + 1] = bf2f_hi(ra[j]);
    c[2 * j] = bf2f(rb[j] & 0xffffu); c[2 * j + 1] = bf2f_hi(rb[j]);
  }
  ln_pair(a, c);
  STAMP(3);
  f2* L = lds + w * WSTR;
  unsigned* lp = (unsigned*)L;
  u4 p0, p1;
#pragma unroll
  for (int j = 0; j < 4; ++j) {
    p0[j] = (__float_as_uint(a[j]) >> 16) | (__float_as_uint(c[j]) & 0xffff0000u);
    p1[j] = (__float_as_uint(a[j + 4]) >> 16) | (__float_as_uint(c[j + 4]) & 0xffff0000u);
  }
  *(u4*)(lp + l * 8) = p0;
  *(u4*)(lp + l * 8 + 4) = p1;
  WAVE_BAR();
  f2 v[8];
#pragma unroll
  for (int m = 0; m < 8; ++m) { unsigned q = lp[l + 64 * m]; v[m] = (f2){bf2f(q & 0xffffu), bf2f_hi(q)}; }
  WAVE_BAR();
  dft8<false>(v);
#pragma unroll
  for (int k = 1; k < 8; ++k) v[k] = cmul(v[k], t1[k]);
#pragma unroll
  for (int k = 0; k < 8; ++k) L[k * 72 + l] = v[k];
  WAVE_BAR();
  const int hi = l >> 3, lo = l & 7;
#pragma unroll
  for (int n = 0; n < 8; ++n) v[n] = L[hi * 72 + n * 8 + lo];
  WAVE_BAR();
  dft8<false>(v);
#pragma unroll
  for (int k = 1; k < 8; ++k) v[k] = cmul(v[k], t2[k]);
#pragma unroll
  for (int k = 0; k < 8; ++k) L[hi * 72 + k * 9 + lo] = v[k];
  WAVE_BAR();
#pragma unroll
  for (int n = 0; n < 8; ++n) v[n] = L[hi * 72 + lo * 9 + n];
  WAVE_BAR();
  dft8<false>(v);
#pragma unroll
  for (int k = 0; k < 8; ++k) L[hi + 9 * lo + 72 * k] = v[k];
  STAMP(4);
  __syncthreads();
  STAMP(5);
  const int p = tid & 3;
  const f2* Lp = lds + p * WSTR;
#pragma unroll
  for (int i = 0; i < 4; ++i) {
    int k = (tid >> 2) + 64 * i;
    int kk = (512 - k) & 511;
    f2 z = Lp[k + (k >> 3)];
    f2 y = Lp[kk + (kk >> 3)];
    f4 o = (f4){0.5f * (z.x + y.x), 0.5f * (z.y - y.y), 0.5f * (z.y + y.y), 0.5f * (y.x - z.x)};
    if (k == 0) {
      f2 z256 = Lp[256 + 32];
      o = (f4){z.x, z256.x, z.y, z256.y};
    }
    ST16((f4*)(X1 + (size_t)k * 2048 + b * 8 + 2 * p), o);
  }
  STAMP(6);
  STAMP_FLUSH(0);
}

__global__ __launch_bounds__(256) void k2_col(const f2* __restrict__ X1, f2* __restrict__ X2,
                                              const unsigned* __restrict__ Wt,
                                              const us* __restrict__ wg, const us* __restrict__ wu) {
  __shared__ __attribute__((aligned(16))) f2 lds[8 * 288];
  __shared__ __attribute__((aligned(16))) f2 nat[2048];
  if (blockIdx.x >= 256) {
    const char* pg = (const char*)wg + ((blockIdx.x - 256) >> 3) * 88064 + threadIdx.x * 16;
    u4 acc = {0, 0, 0, 0};
    PF_DELAY();
#pragma unroll
    for (int i = 0; i < 21; ++i) acc ^= *(const u4*)(pg + i * 4096);
    if (threadIdx.x < 128) acc ^= *(const u4*)(pg + 21 * 4096);
    asm volatile("" ::"v"(acc));
    return;
  }
  const int c = (blockIdx.x & 7) * 32 + (blockIdx.x >> 3);
  const int t = threadIdx.x;
  const f2* col = X1 + (size_t)c * 2048;
  STAMP_DECL; STAMP(0);
  f2 v[8];
#pragma unroll
  for (int m = 0; m < 8; ++m) v[m] = col[t + 256 * m];
  u4 w0 = *(const u4*)(Wt + (size_t)c * 2048 + t * 8);
  u4 w1 = *(const u4*)(Wt + (size_t)c * 2048 + t * 8 + 4);
  f4 wsp[8];
  if (c == 0) {
#pragma unroll
    for (int e = 0; e < 8; ++e) wsp[e] = ((const f4*)(Wt + (size_t)256 * 2048))[t * 8 + e];
  }
  f2 t1[8], t2[8], t3[8];
  make_tw(t1, t, 2.0f / 2048.0f);
  make_tw(t2, t & 31, 2.0f / 256.0f);
  make_tw(t3, t & 3, 2.0f / 32.0f);
  STAMP(1);
  const int g = t >> 5, lo5 = t & 31;
  f2* R = lds + g * 288;
  dft8<false>(v);
#pragma unroll
  for (int k = 1; k < 8; ++k) v[k] = cmul(v[k], t1[k]);
#pragma unroll
  for (int k = 0; k < 8; ++k) lds[k * 288 + t] = v[k];
  STAMP(2);
  __syncthreads();
#pragma unroll
  for (int n = 0; n < 8; ++n) v[n] = R[n * 32 + lo5];
  WAVE_BAR();
  dft8<false>(v);
#pragma unroll
  for (int k = 1; k < 8; ++k) v[k] = cmul(v[k], t2[k]);
#pragma unroll
  for (int k = 0; k < 8; ++k) R[k * 36 + lo5] = v[k];
  WAVE_BAR();
  const int m3 = (t >> 2) & 7, q3 = t & 3;
#pragma unroll
  for (int n = 0; n < 8; ++n) v[n] = R[m3 * 36 + 4 * n + q3];
  WAVE_BAR();
  dft8<false>(v);
#pragma unroll
  for (int k = 1; k < 8; ++k) v[k] = cmul(v[k], t3[k]);
#pragma unroll
  for (int k = 0; k < 8; ++k) R[m3 * 34 + 4 * k + q3] = v[k];
  WAVE_BAR();
#pragma unroll
  for (int e = 0; e < 8; ++e) v[e] = R[m3 * 34 + 8 * q3 + e];
  WAVE_BAR();
  dft4<false>(v[0], v[1], v[2], v[3]);
  dft4<false>(v[4], v[5], v[6], v[7]);
  STAMP(3);
  if (c == 0) {
#pragma unroll
    for (int e = 0; e < 8; ++e) nat[p_of_q(t * 8 + e)] = v[e];
    __syncthreads();
#pragma unroll
    for (int e = 0; e < 8; ++e) {
      f2 cm = nat[(2048 - p_of_q(t * 8 + e)) & 2047];
      cm.y = -cm.y;
      v[e] = cmul(v[e], (f2){wsp[e].x, wsp[e].y}) + cmul(cm, (f2){wsp[e].z, wsp[e].w});
    }
  } else {
    const float sc = 1.0f / 1048576.0f;
#pragma unroll
    for (int e = 0; e < 4; ++e) {
      v[e] = cmul(v[e], (f2){bf2f(w0[e] & 0xffffu) * sc, bf2f_hi(w0[e]) * sc});
      v[e + 4] = cmul(v[e + 4], (f2){bf2f(w1[e] & 0xffffu) * sc, bf2f_hi(w1[e]) * sc});
    }
  }
  STAMP(4);
  dft4<true>(v[0], v[1], v[2], v[3]);
  dft4<true>(v[4], v[5], v[6], v[7]);
#pragma unroll
  for (int e = 0; e < 8; ++e) R[m3 * 34 + 8 * q3 + e] = v[e];
  WAVE_BAR();
#pragma unroll
  for (int k = 0; k < 8; ++k) v[k] = R[m3 * 34 + 4 * k + q3];
  WAVE_BAR();
#pragma unroll
  for (int k = 1; k < 8; ++k) v[k] = cmulc(v[k], t3[k]);
  dft8<true>(v);
#pragma unroll
  for (int n = 0; n < 8; ++n) R[m3 * 36 + 4 * n + q3] = v[n];
  WAVE_BAR();
#pragma unroll
  for (int k = 0; k < 8; ++k) v[k] = R[k * 36 + lo5];
  WAVE_BAR();
#pragma unroll
  for (int k = 1; k < 8; ++k) v[k] = cmulc(v[k], t2[k]);
  dft8<true>(v);
#pragma unroll
  for (int n = 0; n < 8; ++n) R[n * 32 + lo5] = v[n];
  __syncthreads();
#pragma unroll
  for (int k = 0; k < 8; ++k) v[k] = lds[k * 288 + t];
#pragma unroll
  for (int k = 1; k < 8; ++k) v[k] = cmulc(v[k], t1[k]);
  dft8<true>(v);
  STAMP(5);
  f2* ocol = X2 + (size_t)c * 2048;
#pragma unroll
  for (int m = 0; m < 8; ++m) ST8(ocol + t + 256 * m, v[m]);
  STAMP(6);
  STAMP_FLUSH(1);
}

__global__ __launch_bounds__(256) void k3_ifft(const f2* __restrict__ X2, const us* __restrict__ x,
                                               us* __restrict__ h, us* __restrict__ ln2,
                                               const us* __restrict__ wg, const us* __restrict__ wu) {
  __shared__ __attribute__((aligned(16))) f2 lds[4 * WSTR];
  const int tid = threadIdx.x;
  if (blockIdx.x >= 256) {
    const char* pu = (const char*)wu + ((blockIdx.x - 256) >> 3) * 88064 + threadIdx.x * 16;
    u4 acc = {0, 0, 0, 0};
    PF_DELAY();
#pragma unroll
    for (int i = 0; i < 21; ++i) acc ^= *(const u4*)(pu + i * 4096);
    if (threadIdx.x < 128) acc ^= *(const u4*)(pu + 21 * 4096);
    asm volatile("" ::"v"(acc));
    return;
  }
  const int b = (blockIdx.x & 7) * 32 + (blockIdx.x >> 3);
  const int w = tid >> 6, l = tid & 63;
  const int p = tid & 3;
  STAMP_DECL; STAMP(0);
  f4 o[4];
#pragma unroll
  for (int i = 0; i < 4; ++i) {
    int k = (tid >> 2) + 64 * i;
    o[i] = *(const f4*)(X2 + (size_t)k * 2048 + b * 8 + 2 * p);
  }
  const int row = b * 8 + w * 2;
  u4 ra = *(const u4*)(x + (size_t)row * 512 + l * 8);
  u4 rb = *(const u4*)(x + (size_t)(row + 1) * 512 + l * 8);
  f2 t1[8], t2[8];
  make_tw(t1, l, 2.0f / 512.0f);
  make_tw(t2, l & 7, 2.0f / 64.0f);
  STAMP(1); STAMPW(2);
  f2* Lp = lds + p * WSTR;
#pragma unroll
  for (int i = 0; i < 4; ++i) {
    int k = (tid >> 2) + 64 * i;
    int kk = (512 - k) & 511;
    f4 z = o[i];
    if (k == 0) {
      Lp[0] = (f2){z.x, z.z};
      Lp[256 + 32] = (f2){z.y, z.w};
    } else {
      Lp[k + (k >> 3)] = (f2){z.x - z.w, z.y + z.z};
      Lp[kk + (kk >> 3)] = (f2){z.x + z.w, z.z - z.y};
    }
  }
  __syncthreads();
  STAMP(3);
  f2* L = lds + w * WSTR;
  const int hi = l >> 3, lo = l & 7;
  f2 v[8];
#pragma unroll
  for (int k = 0; k < 8; ++k) v[k] = L[hi + 9 * lo + 72 * k];
  WAVE_BAR();
  dft8<true>(v);
#pragma unroll
  for (int n = 1; n < 8; ++n) v[n] = cmulc(v[n], t2[n]);
#pragma unroll
  for (int n = 0; n < 8; ++n) L[hi * 72 + lo * 9 + n] = v[n];
  WAVE_BAR();
#pragma unroll
  for (int k = 0; k < 8; ++k) v[k] = L[hi * 72 + k * 9 + lo];
  WAVE_BAR();
  dft8<true>(v);
#pragma unroll
  for (int n = 0; n < 8; ++n) L[hi * 72 + n * 8 + lo] = v[n];
  WAVE_BAR();
#pragma unroll
  for (int k = 0; k < 8; ++k) v[k] = L[k * 72 + l];
  WAVE_BAR();
#pragma unroll
  for (int k = 1; k < 8; ++k) v[k] = cmulc(v[k], t1[k]);
  dft8<true>(v);
  STAMP(4);
  unsigned* lp = (unsigned*)L;
#pragma unroll
  for (int m = 0; m < 8; ++m) lp[l + 64 * m] = pack2(v[m].x, v[m].y);
  WAVE_BAR();
  u4 y0 = *(const u4*)(lp + l * 8), y1 = *(const u4*)(lp + l * 8 + 4);
  float a[8], c[8];
#pragma unroll
  for (int j = 0; j < 4; ++j) {
    unsigned ya = y0[j], yb = y1[j];
    float xa0 = (j & 1) ? bf2f_hi(ra[j >> 1]) : bf2f(ra[j >> 1] & 0xffffu);
    float xb0 = (j & 1) ? bf2f_hi(rb[j >> 1]) : bf2f(rb[j >> 1] & 0xffffu);
    float xa1 = (j & 1) ? bf2f_hi(ra[2 + (j >> 1)]) : bf2f(ra[2 + (j >> 1)] & 0xffffu);
    float xb1 = (j & 1) ? bf2f_hi(rb[2 + (j >> 1)]) : bf2f(rb[2 + (j >> 1)] & 0xffffu);
    a[j] = rbf(xa0 + bf2f(ya & 0xffffu));
    c[j] = rbf(xb0 + bf2f_hi(ya));
    a[j + 4] = rbf(xa1 + bf2f(yb & 0xffffu));
    c[j + 4] = rbf(xb1 + bf2f_hi(yb));
  }
  u4 ha, hb;
#pragma unroll
  for (int j = 0; j < 4; ++j) {
    ha[j] = (__float_as_uint(a[2 * j]) >> 16) | (__float_as_uint(a[2 * j + 1]) & 0xffff0000u);
    hb[j] = (__float_as_uint(c[2 * j]) >> 16) | (__float_as_uint(c[2 * j + 1]) & 0xffff0000u);
  }
  *(u4*)(h + (size_t)row * 512 + l * 8) = ha;
  *(u4*)(h + (size_t)(row + 1) * 512 + l * 8) = hb;
  ln_pair(a, c);
#pragma unroll
  for (int j = 0; j < 4; ++j) {
    ha[j] = (__float_as_uint(a[2 * j]) >> 16) | (__float_as_uint(a[2 * j + 1]) & 0xffff0000u);
    hb[j] = (__float_as_uint(c[2 * j]) >> 16) | (__float_as_uint(c[2 * j + 1]) & 0xffff0000u);
  }
  {
    int mt = row >> 7, rbk = (row >> 4) & 7, kb = l >> 2, ch = l & 3;
    size_t base = (((size_t)mt * 16 + kb) * 8 + rbk) * 64 + 16 * ch;
    *(u4*)(ln2 + (base + (row & 15)) * 8) = ha;
    *(u4*)(ln2 + (base + ((row + 1) & 15)) * 8) = hb;
  }
  STAMP(5);
  STAMP_FLUSH(2);
}

#define EXP_PD(x) (x)
#define GLDS(gp, lp) __builtin_amdgcn_global_load_lds((const unsigned*)(gp), (unsigned*)(lp), 16, 0, 0)
#define WAIT_V(n) asm volatile("s_waitcnt vmcnt(" #n ")" ::: "memory")
#define WAIT_L0() asm volatile("s_waitcnt lgkmcnt(0)" ::: "memory")
#define RAW_BAR() do { asm volatile("s_waitcnt lgkmcnt(0)" ::: "memory"); __builtin_amdgcn_s_barrier(); } while (0)
typedef short s4v __attribute__((ext_vector_type(4)));
#define LDS_TR(dst, addr, off) asm volatile("ds_read_b64_tr_b16 %0, %1 offset:%2" : "=v"(dst) : "v"(addr), "i"(off))
#define LDS_B128(dst, addr, off) asm volatile("ds_read_b128 %0, %1 offset:%2" : "=v"(dst) : "v"(addr), "i"(off))
#define WAIT_L(n) asm volatile("s_waitcnt lgkmcnt(" #n ")" ::: "memory")
#define CAT8(a, b) ((bf8){a[0], a[1], a[2], a[3], b[0], b[1], b[2], b[3]})
#define SCHED0() __builtin_amdgcn_sched_barrier(0)

__device__ __forceinline__ float gelu_t(float x) {
  float y2 = -2.3022082007793f * (x + 0.044715f * x * x * x);
  return x * __builtin_amdgcn_rcpf(1.0f + __builtin_amdgcn_exp2f(y2));
}

__global__ __launch_bounds__(512) void k4_gemm1(const us* __restrict__ ln2, const us* __restrict__ wg,
                                                const us* __restrict__ wu, const us* __restrict__ wd,
                                                us* __restrict__ act) {
  __shared__ __attribute__((aligned(16))) char sm[3 * 40960 + 8192];
  const int tid = threadIdx.x, wid = tid >> 6, lane = tid & 63;
  const int xcd = blockIdx.x & 7, idx = blockIdx.x >> 3;
  const int mt = 2 * xcd + (idx >> 4), ft = idx & 15;
  const int wm = wid >> 1, wn = wid & 1;
  STAMP_DECL; STAMP(0);
  const char* gA = (const char*)ln2 + (size_t)mt * 131072 + tid * 16;
  const char* gB[3];
#pragma unroll
  for (int i = 0; i < 3; ++i) {
    int q = i * 512 + tid;
    int mat = q >= 768 ? 1 : 0;
    int qq = q - mat * 768;
    int r = qq / 12, cp = qq - r * 12;
    int cch = cp ^ (((r >> 3) & 1) << 1);
    int col0 = 88 * ft + 8 * cch;
    col0 = col0 > 1368 ? 1368 : col0;
    gB[i] = (const char*)((mat ? wu : wg) + (size_t)r * 1376 + col0);
  }
  auto stage = [&](int s, int buf) {
    char* dst = sm + buf * 40960 + wid * 1024;
    GLDS(gA + s * 16384, dst);
    GLDS(gA + s * 16384 + 8192, dst + 8192);
    GLDS(gB[0] + (size_t)s * 64 * 2752, dst + 16384);
    GLDS(gB[1] + (size_t)s * 64 * 2752, dst + 16384 + 8192);
    GLDS(gB[2] + (size_t)s * 64 * 2752, dst + 16384 + 16384);
  };
  f4 ag[2][3], au[2][3];
#pragma unroll
  for (int i = 0; i < 2; ++i)
#pragma unroll
    for (int f = 0; f < 3; ++f) { ag[i][f] = (f4){0, 0, 0, 0}; au[i][f] = (f4){0, 0, 0, 0}; }
  STAMP(12);
  stage(0, 0);
  STAMP(13);
  __builtin_amdgcn_s_barrier();
  stage(1, 1);
  stage(2, 2);
  STAMP(14);
  const int g = lane >> 4, q4 = (lane & 15) >> 2, p4 = lane & 3;
  const unsigned smb = (unsigned)(size_t)sm;
  const unsigned tb = smb + 16384 + (8 * g + q4) * 192 + 8 * (p4 & 1) + 16 * (p4 >> 1) + 96 * wn;
  const int sgn = 32 * (g & 1);
  const unsigned tb0 = tb + (wn ? -sgn : sgn), tb1 = tb + (wn ? sgn : -sgn);
  const unsigned ab = smb + lane * 16 + wm * 2048;
  bf8 xb[2][2][2];
  s4v wl[2][2][2][3][2];
#define RD1(st, set, r) do {                                                                        \
    const unsigned bo = ((st) % 3) * 40960;                                                         \
    constexpr int kk_ = (r) / 14, e_ = (r) % 14;                                                    \
    if constexpr (e_ < 2) { LDS_B128(xb[set][kk_][e_], ab + bo, kk_ * 8192 + e_ * 1024); }          \
    else { constexpr int m_ = (e_ - 2) / 6, f_ = ((e_ - 2) % 6) / 2, t_ = (e_ - 2) % 2;             \
      LDS_TR(wl[set][kk_][m_][f_][t_], ((f_ & 1) ? tb1 : tb0) + bo, kk_ * 6144 + m_ * 12288 + f_ * 32 + t_ * 768); } \
  } while (0)
#define RD4(st, set, r) do { RD1(st, set, r); RD1(st, set, r + 1); RD1(st, set, r + 2); RD1(st, set, r + 3); } while (0)
  WAIT_V(10);
  RAW_BAR();
  STAMP(1);
  RD4(0, 0, 0); RD4(0, 0, 4); RD4(0, 0, 8); RD4(0, 0, 12); RD4(0, 0, 16); RD4(0, 0, 20); RD4(0, 0, 24);
  const char* pd = (const char*)wd + (size_t)idx * 44032 + tid * 16;
  char* dump = sm + 3 * 40960 + wid * 1024;
#pragma unroll
  for (int s = 0; s < 8; ++s) {
    const int cur = s & 1, nx = cur ^ 1;
    if (s < 6) WAIT_V(5); else if (s == 6) WAIT_V(6);
    RAW_BAR();
    SCHED0();
    STAMP(2 + s);
    char* dst = sm + (s % 3) * 40960 + wid * 1024;
#define MM(kk, f, i) do {                                                                                   \
      ag[i][f] = __builtin_amdgcn_mfma_f32_16x16x32_bf16(CAT8(wl[cur][kk][0][f][0], wl[cur][kk][0][f][1]),    \
                                                         xb[cur][kk][i], ag[i][f], 0, 0, 0);                \
      au[i][f] = __builtin_amdgcn_mfma_f32_16x16x32_bf16(CAT8(wl[cur][kk][1][f][0], wl[cur][kk][1][f][1]),    \
                                                         xb[cur][kk][i], au[i][f], 0, 0, 0); } while (0)
#define DMA(j) do {                                                                                  \
      if (s + 3 < 8) {                                                                               \
        if (j == 0) GLDS(gA + (s + 3) * 16384, dst);                                                 \
        if (j == 1) GLDS(gA + (s + 3) * 16384 + 8192, dst + 8192);                                   \
        if (j >= 2 && j < 5) GLDS(gB[j >= 2 ? j - 2 : 0] + (size_t)(s + 3) * 64 * 2752, dst + 16384 + (j - 2) * 8192); \
      } else if (s == 5) {                                                                           \
        if (j < 5) GLDS(pd + j * 8192, dump);                                                        \
        else GLDS(pd + (tid < 192 ? 5 * 8192 : 0), dump);                                            \
      } } while (0)
#define RDN(r) do { if (s + 1 < 8) { RD1(s + 1, nx, r); RD1(s + 1, nx, r + 1); } } while (0)
#define RDN3(r) do { if (s + 1 < 8) { RD1(s + 1, nx, r); RD1(s + 1, nx, r + 1); RD1(s + 1, nx, r + 2); } } while (0)
    MM(0, 0, 0); SCHED0(); DMA(0); RDN(0);   SCHED0();
    MM(0, 0, 1); SCHED0(); RDN3(2);          SCHED0();
    MM(0, 1, 0); SCHED0(); DMA(1); RDN(5);   SCHED0();
    MM(0, 1, 1); SCHED0(); RDN3(7);          SCHED0();
    MM(0, 2, 0); SCHED0(); DMA(2); RDN(10);  SCHED0();
    MM(0, 2, 1); SCHED0(); RDN3(12);         SCHED0();
    MM(1, 0, 0); SCHED0(); DMA(3); RDN(15);  SCHED0();
    MM(1, 0, 1); SCHED0(); RDN3(17);         SCHED0();
    MM(1, 1, 0); SCHED0(); DMA(4); RDN(20);  SCHED0();
    MM(1, 1, 1); SCHED0(); RDN3(22);         SCHED0();
    MM(1, 2, 0); SCHED0(); DMA(5); RDN3(25); SCHED0();
    MM(1, 2, 1); SCHED0();
  }
  STAMP(10);
#pragma unroll
  for (int i = 0; i < 2; ++i) {
    int row = mt * 128 + wm * 32 + i * 16 + (lane & 15);
    int mt2 = row >> 6, rbl = (row >> 4) & 3;
#pragma unroll
    for (int f = 0; f < 3; ++f) {
      int c = wn * 48 + f * 16 + 4 * (lane >> 4);
      int k = 88 * ft + c;
      float r[4];
#pragma unroll
      for (int j = 0; j < 4; ++j) {
        float gg = rbf(ag[i][f][j]), uu = rbf(au[i][f][j]);
        r[j] = rbf(gelu_t(gg)) * uu;
      }
      u2 o = (u2){pack2(r[0], r[1]), pack2(r[2], r[3])};
      size_t off = ((((size_t)mt2 * 43 + (k >> 5)) * 4 + rbl) * 64 + (lane & 15) + 16 * ((k & 31) >> 3)) * 8 + (k & 7);
      if (c < 88 && k < 1376) *(u2*)(act + off) = o;
    }
  }
  WAIT_V(0);
  STAMP(11);
  STAMP_FLUSH(3);
}

__global__ __launch_bounds__(512) void k5_gemm2(const us* __restrict__ act, const us* __restrict__ wd,
                                                const us* __restrict__ h, us* __restrict__ out) {
  __shared__ __attribute__((aligned(16))) char sm[131072];
  const int tid = threadIdx.x, wid = tid >> 6, lane = tid & 63;
  const int xcd = blockIdx.x & 7, idx = blockIdx.x >> 3;
  const int mt2 = xcd * 4 + (idx >> 3), nt = idx & 7;
  STAMP_DECL; STAMP(0);
  char* ring = sm + wid * 16384;
  const char* gA = (const char*)act + ((size_t)mt2 * 43 * 4) * 1024 + lane * 16;
  const char* gW[4];
#pragma unroll
  for (int i = 0; i < 4; ++i) {
    int rho = 8 * i + (lane >> 3);
    int sw = ((rho >> 1) & 1) | (((rho >> 3) & 1) << 1);
    int ch = (lane & 7) ^ (sw << 1);
    gW[i] = (const char*)(wd + (size_t)rho * 512 + nt * 64 + ch * 8);
  }
  auto stage = [&](int kb, int buf) {
    char* dst = ring + buf * 8192;
#pragma unroll
    for (int i = 0; i < 4; ++i) GLDS(gA + (size_t)kb * 4096 + i * 1024, dst + i * 1024);
#pragma unroll
    for (int i = 0; i < 4; ++i) GLDS(gW[i] + (size_t)kb * 32 * 1024, dst + 4096 + i * 1024);
  };
  f4 acc[4][4];
#pragma unroll
  for (int i = 0; i < 4; ++i)
#pragma unroll
    for (int j = 0; j < 4; ++j) acc[i][j] = (f4){0, 0, 0, 0};
  const int g = lane >> 4, q4 = (lane & 15) >> 2, p4 = lane & 3;
  const int sw = ((q4 >> 1) & 1) | ((g & 1) << 1);
  const unsigned ringb = (unsigned)(size_t)sm + wid * 16384;
  const unsigned trb = ringb + 4096 + (8 * g + q4) * 128 + 8 * (p4 & 1) + 16 * (p4 >> 1);
  const unsigned tr0 = trb + 32 * (0 ^ sw), tr1 = trb + 32 * (1 ^ sw), tr2 = trb + 32 * (2 ^ sw), tr3 = trb + 32 * (3 ^ sw);
  const unsigned ab = ringb + lane * 16;
  const int nst = wid < 3 ? 6 : 5;
  stage(wid, 0);
  stage(wid + 8, 1);
#pragma unroll
  for (int i = 0; i < 6; ++i) {
    if (i < nst) {
      if (i + 1 < nst) WAIT_V(8); else WAIT_V(0);
      bf8 xa[4];
      s4v wl[4][2];
      LDS_B128(xa[0], ab, (i & 1) * 8192 + 0);
      LDS_B128(xa[1], ab, (i & 1) * 8192 + 1024);
      LDS_B128(xa[2], ab, (i & 1) * 8192 + 2048);
      LDS_B128(xa[3], ab, (i & 1) * 8192 + 3072);
      LDS_TR(wl[0][0], tr0, (i & 1) * 8192); LDS_TR(wl[0][1], tr0, (i & 1) * 8192 + 512);
      LDS_TR(wl[1][0], tr1, (i & 1) * 8192); LDS_TR(wl[1][1], tr1, (i & 1) * 8192 + 512);
      LDS_TR(wl[2][0], tr2, (i & 1) * 8192); LDS_TR(wl[2][1], tr2, (i & 1) * 8192 + 512);
      LDS_TR(wl[3][0], tr3, (i & 1) * 8192); LDS_TR(wl[3][1], tr3, (i & 1) * 8192 + 512);
      WAIT_L(0);
      SCHED0();
      if (i + 2 < nst) stage(wid + 8 * (i + 2), i & 1);
#pragma unroll
      for (int nb = 0; nb < 4; ++nb) {
        bf8 wb = CAT8(wl[nb][0], wl[nb][1]);
#pragma unroll
        for (int rb = 0; rb < 4; ++rb)
          acc[nb][rb] = __builtin_amdgcn_mfma_f32_16x16x32_bf16(wb, xa[rb], acc[nb][rb], 0, 0, 0);
      }
      SCHED0();
    }
  }
  STAMP(1);
  f4* red = (f4*)sm;
#pragma unroll
  for (int nb = 0; nb < 4; ++nb)
#pragma unroll
    for (int rb = 0; rb < 4; ++rb) red[(wid * 16 + nb * 4 + rb) * 64 + lane] = acc[nb][rb];
  __syncthreads();
  STAMP(2);
  const int rb = wid >> 1;
#pragma unroll
  for (int q = 0; q < 2; ++q) {
    int nb = 2 * (wid & 1) + q;
    f4 s = red[(nb * 4 + rb) * 64 + lane];
#pragma unroll
    for (int src = 1; src < 8; ++src) s += red[(src * 16 + nb * 4 + rb) * 64 + lane];
    int token = mt2 * 64 + rb * 16 + (lane & 15);
    int col = nt * 64 + nb * 16 + 4 * (lane >> 4);
    u2 hv = *(const u2*)(h + (size_t)token * 512 + col);
    float o0 = bf2f(hv[0] & 0xffffu) + rbf(s[0]);
    float o1 = bf2f_hi(hv[0]) + rbf(s[1]);
    float o2 = bf2f(hv[1] & 0xffffu) + rbf(s[2]);
    float o3 = bf2f_hi(hv[1]) + rbf(s[3]);
    *(u2*)(out + (size_t)token * 512 + col) = (u2){pack2(o0, o1), pack2(o2, o3)};
  }
  STAMP(3);
  STAMP_FLUSH(4);
}

extern "C" void kernel_launch(void* const* d_in, const int* in_sizes, int n_in, void* d_out, int out_size,
                              void* d_ws, size_t ws_size, hipStream_t stream) {
  const us* x = (const us*)d_in[0];
  const us* wr = (const us*)d_in[1];
  const us* wi = (const us*)d_in[2];
  const us* wg = (const us*)d_in[4];
  const us* wu = (const us*)d_in[5];
  const us* wd = (const us*)d_in[6];
  char* ws = (char*)d_ws;
  f2* X1 = (f2*)(ws);
  f2* X2 = (f2*)(ws + (5u << 20));
  us* h = (us*)(ws + (10u << 20));
  us* ln2 = (us*)(ws + (12u << 20));
  us* act = (us*)(ws + (14u << 20));
  unsigned* Wt = (unsigned*)(ws + (20u << 20));
  k1_lnfft<<<512, 256, 0, stream>>>(x, X1, wr, wi, Wt);
  k2_col<<<256 + 128, 256, 0, stream>>>(X1, X2, Wt, wg, wu);
  k3_ifft<<<256 + 128, 256, 0, stream>>>(X2, x, h, ln2, wg, wu);
  k4_gemm1<<<256, 512, 0, stream>>>(ln2, wg, wu, wd, act);
  k5_gemm2<<<256, 512, 0, stream>>>(act, wd, h, (us*)d_out);
}
```

```cpp
#include <hip/hip_runtime.h>
#include <hip/hip_bf16.h>
#include <stdint.h>

typedef float f2 __attribute__((ext_vector_type(2)));
typedef float f4 __attribute__((ext_vector_type(4)));
typedef unsigned u4 __attribute__((ext_vector_type(4)));
typedef unsigned u2 __attribute__((ext_vector_type(2)));
typedef short bf8 __attribute__((ext_vector_type(8)));
typedef unsigned short us;

#define WAVE_BAR() __builtin_amdgcn_wave_barrier()
#define ST16(ptr, val) asm volatile("global_store_dwordx4 %0, %1, off sc1\n\ts_nop 1" :: "v"(ptr), "v"(val) : "memory")
#define ST8(ptr, val) asm volatile("global_store_dwordx2 %0, %1, off sc1\n\ts_nop 1" :: "v"(ptr), "v"(val) : "memory")
#define STAMP_DECL
#define STAMP(i)
#define STAMPW(i)
#define STAMP_FLUSH(kid)

__device__ __forceinline__ float bf2f(unsigned h) { return __uint_as_float(h << 16); }
__device__ __forceinline__ float bf2f_hi(unsigned h) { return __uint_as_float(h & 0xffff0000u); }
__device__ __forceinline__ unsigned f2bf(float f) {
  __bf16 b = (__bf16)f;
  return (unsigned)__builtin_bit_cast(unsigned short, b);
}
__device__ __forceinline__ float rbf(float f) { return bf2f(f2bf(f)); }
__device__ __forceinline__ unsigned pack2(float lo, float hi) { return f2bf(lo) | (f2bf(hi) << 16); }

__device__ __forceinline__ f2 cmul(f2 a, f2 b) { return (f2){a.x * b.x - a.y * b.y, a.x * b.y + a.y * b.x}; }
__device__ __forceinline__ f2 cmulc(f2 a, f2 b) { return (f2){a.x * b.x + a.y * b.y, a.y * b.x - a.x * b.y}; }
template <bool INV> __device__ __forceinline__ f2 rot(f2 a) { return INV ? (f2){-a.y, a.x} : (f2){a.y, -a.x}; }

template <bool INV> __device__ __forceinline__ void dft8(f2 (&v)[8]) {
  const float r = 0.70710678118654752f;
  f2 a0 = v[0] + v[4], a1 = v[0] - v[4], a2 = v[2] + v[6], a3 = rot<INV>(v[2] - v[6]);
  f2 a4 = v[1] + v[5], a5 = v[1] - v[5], a6 = v[3] + v[7], a7 = rot<INV>(v[3] - v[7]);
  f2 e0 = a0 + a2, e2 = a0 - a2, e1 = a1 + a3, e3 = a1 - a3;
  f2 o0 = a4 + a6, o2 = a4 - a6, o1 = a5 + a7, o3 = a5 - a7;
  o1 = (o1 + rot<INV>(o1)) * r;
  o2 = rot<INV>(o2);
  o3 = (rot<INV>(o3) - o3) * r;
  v[0] = e0 + o0; v[4] = e0 - o0;
  v[1] = e1 + o1; v[5] = e1 - o1;
  v[2] = e2 + o2; v[6] = e2 - o2;
  v[3] = e3 + o3; v[7] = e3 - o3;
}
template <bool INV> __device__ __forceinline__ void dft4(f2& x0, f2& x1, f2& x2, f2& x3) {
  f2 a0 = x0 + x2, a1 = x0 - x2, a2 = x1 + x3, a3 = rot<INV>(x1 - x3);
  x0 = a0 + a2; x2 = a0 - a2; x1 = a1 + a3; x3 = a1 - a3;
}
__device__ __forceinline__ void make_tw(f2 (&t)[8], int num, float c2) {
  float s, c;
  sincospif(-(float)num * c2, &s, &c);
  t[1] = (f2){c, s};
  t[2] = cmul(t[1], t[1]);
  t[3] = cmul(t[2], t[1]);
  t[4] = cmul(t[2], t[2]);
  t[5] = cmul(t[4], t[1]);
  t[6] = cmul(t[3], t[3]);
  t[7] = cmul(t[4], t[3]);
}

template <int ctrl, int rm> __device__ __forceinline__ float dpp_add(float v) {
  int t = __builtin_amdgcn_update_dpp(0, __float_as_int(v), ctrl, rm, 0xf, false);
  return v + __int_as_float(t);
}
__device__ __forceinline__ float wave_sum(float v) {
  v = dpp_add<0x111, 0xf>(v);
  v = dpp_add<0x112, 0xf>(v);
  v = dpp_add<0x114, 0xf>(v);
  v = dpp_add<0x118, 0xf>(v);
  v = dpp_add<0x142, 0xa>(v);
  v = dpp_add<0x143, 0xc>(v);
  return __int_as_float(__builtin_amdgcn_readlane(__float_as_int(v), 63));
}

__device__ __forceinline__ void ln_pair(float (&a)[8], float (&b)[8]) {
  float sa = 0.f, sb = 0.f;
#pragma unroll
  for (int j = 0; j < 8; ++j) { sa += a[j]; sb += b[j]; }
  sa = wave_sum(sa); sb = wave_sum(sb);
  float ma = sa * (1.0f / 512.0f), mb = sb * (1.0f / 512.0f);
  float va = 0.f, vb = 0.f;
#pragma unroll
  for (int j = 0; j < 8; ++j) { float d = a[j] - ma; va += d * d; float e = b[j] - mb; vb += e * e; }
  va = wave_sum(va) * (1.0f / 512.0f); vb = wave_sum(vb) * (1.0f / 512.0f);
  float mra = rbf(ma), mrb = rbf(mb);
  float da = rbf(sqrtf(rbf(rbf(va) + 1e-5f))), db = rbf(sqrtf(rbf(rbf(vb) + 1e-5f)));
  const float ra_ = 1.0f / da, rb_ = 1.0f / db;
#pragma unroll
  for (int j = 0; j < 8; ++j) {
    float na = rbf(a[j] - mra), nb = rbf(b[j] - mrb);
    float qa = na * ra_, qb = nb * rb_;
    qa = __builtin_fmaf(__builtin_fmaf(-qa, da, na), ra_, qa);
    qb = __builtin_fmaf(__builtin_fmaf(-qb, db, nb), rb_, qb);
    a[j] = rbf(qa); b[j] = rbf(qb);
  }
}

__device__ __forceinline__ int p_of_q(int q) {
  int T = q >> 3, e = q & 7;
  int k0 = T >> 5, k1 = (T >> 2) & 7, k2 = 2 * (T & 3) + (e >> 2), k3 = e & 3;
  return k0 + 8 * k1 + 64 * k2 + 512 * k3;
}

#define PFD 24
#define PF_DELAY() do { for (int i_ = 0; i_ < PFD; ++i_) __builtin_amdgcn_s_sleep(2); } while (0)
#define WSTR 584

__global__ __launch_bounds__(256) void k1_lnfft(const us* __restrict__ x, f2* __restrict__ X1,
                                                const us* __restrict__ wr, const us* __restrict__ wi,
                                                unsigned* __restrict__ Wt) {
  __shared__ __attribute__((aligned(16))) f2 lds[4 * WSTR];
  const int tid = threadIdx.x;
  if (blockIdx.x >= 256) {
    unsigned* tile = (unsigned*)lds;
    const int xg = blockIdx.x & 7, qi = (blockIdx.x - 256) >> 3;
    const int c = tid & 31;
#pragma unroll
    for (int i = 0; i < 8; ++i) {
      int qq = (tid >> 5) + 8 * i;
      int pp = p_of_q(64 * qi + qq);
      unsigned r_ = wr[pp * 257 + 32 * xg + c], i_ = wi[pp * 257 + 32 * xg + c];
      tile[c * 65 + qq] = r_ | (i_ << 16);
    }
    if (xg == 0 && tid < 64) {
      int pp = p_of_q(64 * qi + tid);
      Wt[(size_t)256 * 2048 + 64 * qi + tid] = (unsigned)wr[pp * 257 + 256] | ((unsigned)wi[pp * 257 + 256] << 16);
    }
    __syncthreads();
#pragma unroll
    for (int i = 0; i < 8; ++i) {
      int cc = (tid >> 6) + 4 * i;
      Wt[(size_t)(32 * xg + cc) * 2048 + 64 * qi + (tid & 63)] = tile[cc * 65 + (tid & 63)];
    }
    return;
  }
  const int b = (blockIdx.x & 7) * 32 + (blockIdx.x >> 3);
  const int w = tid >> 6, l = tid & 63;
  STAMP_DECL; STAMP(0);
  const int row = b * 8 + w * 2;
  u4 ra = *(const u4*)(x + (size_t)row * 512 + l * 8);
  u4 rb = *(const u4*)(x + (size_t)(row + 1) * 512 + l * 8);
  f2 t1[8], t2[8];
  make_tw(t1, l, 2.0f / 512.0f);
  make_tw(t2, l & 7, 2.0f / 64.0f);
  STAMP(1); STAMPW(2);
  float a[8], c[8];
#pragma unroll
  for (int j = 0; j < 4; ++j) {
    a[2 * j] = bf2f(ra[j] & 0xffffu); a[2 * j + 1] = bf2f_hi(ra[j]);
    c[2 * j] = bf2f(rb[j] & 0xffffu); c[2 * j + 1] = bf2f_hi(rb[j]);
  }
  ln_pair(a, c);
  STAMP(3);
  f2* L = lds + w * WSTR;
  unsigned* lp = (unsigned*)L;
  u4 p0, p1;
#pragma unroll
  for (int j = 0; j < 4; ++j) {
    p0[j] = (__float_as_uint(a[j]) >> 16) | (__float_as_uint(c[j]) & 0xffff0000u);
    p1[j] = (__float_as_uint(a[j + 4]) >> 16) | (__float_as_uint(c[j + 4]) & 0xffff0000u);
  }
  *(u4*)(lp + l * 8) = p0;
  *(u4*)(lp + l * 8 + 4) = p1;
  WAVE_BAR();
  f2 v[8];
#pragma unroll
  for (int m = 0; m < 8; ++m) { unsigned q = lp[l + 64 * m]; v[m] = (f2){bf2f(q & 0xffffu), bf2f_hi(q)}; }
  WAVE_BAR();
  dft8<false>(v);
#pragma unroll
  for (int k = 1; k < 8; ++k) v[k] = cmul(v[k], t1[k]);
#pragma unroll
  for (int k = 0; k < 8; ++k) L[k * 72 + l] = v[k];
  WAVE_BAR();
  const int hi = l >> 3, lo = l & 7;
#pragma unroll
  for (int n = 0; n < 8; ++n) v[n] = L[hi * 72 + n * 8 + lo];
  WAVE_BAR();
  dft8<false>(v);
#pragma unroll
  for (int k = 1; k < 8; ++k) v[k] = cmul(v[k], t2[k]);
#pragma unroll
  for (int k = 0; k < 8; ++k) L[hi * 72 + k * 9 + lo] = v[k];
  WAVE_BAR();
#pragma unroll
  for (int n = 0; n < 8; ++n) v[n] = L[hi * 72 + lo * 9 + n];
  WAVE_BAR();
  dft8<false>(v);
#pragma unroll
  for (int k = 0; k < 8; ++k) L[hi + 9 * lo + 72 * k] = v[k];
  STAMP(4);
  __syncthreads();
  STAMP(5);
  const int p = tid & 3;
  const f2* Lp = lds + p * WSTR;
#pragma unroll
  for (int i = 0; i < 4; ++i) {
    int k = (tid >> 2) + 64 * i;
    int kk = (512 - k) & 511;
    f2 z = Lp[k + (k >> 3)];
    f2 y = Lp[kk + (kk >> 3)];
    f4 o = (f4){0.5f * (z.x + y.x), 0.5f * (z.y - y.y), 0.5f * (z.y + y.y), 0.5f * (y.x - z.x)};
    ST16((f4*)(X1 + (size_t)k * 2048 + b * 8 + 2 * p), o);
  }
  if (tid < 4) {
    f2 z = Lp[256 + 32];
    *(f4*)(X1 + (size_t)256 * 2048 + b * 8 + 2 * p) = (f4){z.x, 0.f, z.y, 0.f};
  }
  STAMP(6);
  STAMP_FLUSH(0);
}

__global__ __launch_bounds__(256) void k2_col(const f2* __restrict__ X1, f2* __restrict__ X2,
                                              const unsigned* __restrict__ Wt,
                                              const us* __restrict__ wg, const us* __restrict__ wu) {
  __shared__ __attribute__((aligned(16))) f2 lds[8 * 288];
  if (blockIdx.x >= 264) {
    const char* pg = (const char*)wg + ((blockIdx.x - 264) >> 3) * 88064 + threadIdx.x * 16;
    u4 acc = {0, 0, 0, 0};
    PF_DELAY();
#pragma unroll
    for (int i = 0; i < 21; ++i) acc ^= *(const u4*)(pg + i * 4096);
    if (threadIdx.x < 128) acc ^= *(const u4*)(pg + 21 * 4096);
    asm volatile("" ::"v"(acc));
    return;
  }
  if (blockIdx.x > 256) return;
  const int c = blockIdx.x < 256 ? (blockIdx.x & 7) * 32 + (blockIdx.x >> 3) : 256;
  const int t = threadIdx.x;
  const f2* col = X1 + (size_t)c * 2048;
  STAMP_DECL; STAMP(0);
  f2 v[8];
#pragma unroll
  for (int m = 0; m < 8; ++m) v[m] = col[t + 256 * m];
  u4 w0 = *(const u4*)(Wt + (size_t)c * 2048 + t * 8);
  u4 w1 = *(const u4*)(Wt + (size_t)c * 2048 + t * 8 + 4);
  f2 t1[8], t2[8], t3[8];
  make_tw(t1, t, 2.0f / 2048.0f);
  make_tw(t2, t & 31, 2.0f / 256.0f);
  make_tw(t3, t & 3, 2.0f / 32.0f);
  STAMP(1);
  const int g = t >> 5, lo5 = t & 31;
  f2* R = lds + g * 288;
  dft8<false>(v);
#pragma unroll
  for (int k = 1; k < 8; ++k) v[k] = cmul(v[k], t1[k]);
#pragma unroll
  for (int k = 0; k < 8; ++k) lds[k * 288 + t] = v[k];
  STAMP(2);
  __syncthreads();
#pragma unroll
  for (int n = 0; n < 8; ++n) v[n] = R[n * 32 + lo5];
  WAVE_BAR();
  dft8<false>(v);
#pragma unroll
  for (int k = 1; k < 8; ++k) v[k] = cmul(v[k], t2[k]);
#pragma unroll
  for (int k = 0; k < 8; ++k) R[k * 36 + lo5] = v[k];
  WAVE_BAR();
  const int m3 = (t >> 2) & 7, q3 = t & 3;
#pragma unroll
  for (int n = 0; n < 8; ++n) v[n] = R[m3 * 36 + 4 * n + q3];
  WAVE_BAR();
  dft8<false>(v);
#pragma unroll
  for (int k = 1; k < 8; ++k) v[k] = cmul(v[k], t3[k]);
#pragma unroll
  for (int k = 0; k < 8; ++k) R[m3 * 34 + 4 * k + q3] = v[k];
  WAVE_BAR();
#pragma unroll
  for (int e = 0; e < 8; ++e) v[e] = R[m3 * 34 + 8 * q3 + e];
  WAVE_BAR();
  dft4<false>(v[0], v[1], v[2], v[3]);
  dft4<false>(v[4], v[5], v[6], v[7]);
  STAMP(3);
  {
    const float sc = 1.0f / 1048576.0f;
#pragma unroll
    for (int e = 0; e < 4; ++e) {
      v[e] = cmul(v[e], (f2){bf2f(w0[e] & 0xffffu) * sc, bf2f_hi(w0[e]) * sc});
      v[e + 4] = cmul(v[e + 4], (f2){bf2f(w1[e] & 0xffffu) * sc, bf2f_hi(w1[e]) * sc});
    }
  }
  STAMP(4);
  dft4<true>(v[0], v[1], v[2], v[3]);
  dft4<true>(v[4], v[5], v[6], v[7]);
#pragma unroll
  for (int e = 0; e < 8; ++e) R[m3 * 34 + 8 * q3 + e] = v[e];
  WAVE_BAR();
#pragma unroll
  for (int k = 0; k < 8; ++k) v[k] = R[m3 * 34 + 4 * k + q3];
  WAVE_BAR();
#pragma unroll
  for (int k = 1; k < 8; ++k) v[k] = cmulc(v[k], t3[k]);
  dft8<true>(v);
#pragma unroll
  for (int n = 0; n < 8; ++n) R[m3 * 36 + 4 * n + q3] = v[n];
  WAVE_BAR();
#pragma unroll
  for (int k = 0; k < 8; ++k) v[k] = R[k * 36 + lo5];
  WAVE_BAR();
#pragma unroll
  for (int k = 1; k < 8; ++k) v[k] = cmulc(v[k], t2[k]);
  dft8<true>(v);
#pragma unroll
  for (int n = 0; n < 8; ++n) R[n * 32 + lo5] = v[n];
  __syncthreads();
#pragma unroll
  for (int k = 0; k < 8; ++k) v[k] = lds[k * 288 + t];
#pragma unroll
  for (int k = 1; k < 8; ++k) v[k] = cmulc(v[k], t1[k]);
  dft8<true>(v);
  STAMP(5);
  f2* ocol = X2 + (size_t)c * 2048;
#pragma unroll
  for (int m = 0; m < 8; ++m) ST8(ocol + t + 256 * m, v[m]);
  STAMP(6);
  STAMP_FLUSH(1);
}

__global__ __launch_bounds__(256) void k3_ifft(const f2* __restrict__ X2, const us* __restrict__ x,
                                               us* __restrict__ h, us* __restrict__ ln2,
                                               const us* __restrict__ wg, const us* __restrict__ wu) {
  __shared__ __attribute__((aligned(16))) f2 lds[4 * WSTR];
  const int tid = threadIdx.x;
  if (blockIdx.x >= 256) {
    const char* pu = (const char*)wu + ((blockIdx.x - 256) >> 3) * 88064 + threadIdx.x * 16;
    u4 acc = {0, 0, 0, 0};
    PF_DELAY();
#pragma unroll
    for (int i = 0; i < 21; ++i) acc ^= *(const u4*)(pu + i * 4096);
    if (threadIdx.x < 128) acc ^= *(const u4*)(pu + 21 * 4096);
    asm volatile("" ::"v"(acc));
    return;
  }
  const int b = (blockIdx.x & 7) * 32 + (blockIdx.x >> 3);
  const int w = tid >> 6, l = tid & 63;
  const int p = tid & 3;
  STAMP_DECL; STAMP(0);
  f4 o[4];
#pragma unroll
  for (int i = 0; i < 4; ++i) {
    int k = (tid >> 2) + 64 * i;
    o[i] = *(const f4*)(X2 + (size_t)k * 2048 + b * 8 + 2 * p);
  }
  f4 ony = *(const f4*)(X2 + (size_t)256 * 2048 + b * 8 + 2 * p);
  const int row = b * 8 + w * 2;
  u4 ra = *(const u4*)(x + (size_t)row * 512 + l * 8);
  u4 rb = *(const u4*)(x + (size_t)(row + 1) * 512 + l * 8);
  f2 t1[8], t2[8];
  make_tw(t1, l, 2.0f / 512.0f);
  make_tw(t2, l & 7, 2.0f / 64.0f);
  STAMP(1); STAMPW(2);
  f2* Lp = lds + p * WSTR;
#pragma unroll
  for (int i = 0; i < 4; ++i) {
    int k = (tid >> 2) + 64 * i;
    int kk = (512 - k) & 511;
    f4 z = o[i];
    if (k == 0) {
      Lp[0] = (f2){z.x, z.z};
      Lp[256 + 32] = (f2){ony.x, ony.z};
    } else {
      Lp[k + (k >> 3)] = (f2){z.x - z.w, z.y + z.z};
      Lp[kk + (kk >> 3)] = (f2){z.x + z.w, z.z - z.y};
    }
  }
  __syncthreads();
  STAMP(3);
  f2* L = lds + w * WSTR;
  const int hi = l >> 3, lo = l & 7;
  f2 v[8];
#pragma unroll
  for (int k = 0; k < 8; ++k) v[k] = L[hi + 9 * lo + 72 * k];
  WAVE_BAR();
  dft8<true>(v);
#pragma unroll
  for (int n = 1; n < 8; ++n) v[n] = cmulc(v[n], t2[n]);
#pragma unroll
  for (int n = 0; n < 8; ++n) L[hi * 72 + lo * 9 + n] = v[n];
  WAVE_BAR();
#pragma unroll
  for (int k = 0; k < 8; ++k) v[k] = L[hi * 72 + k * 9 + lo];
  WAVE_BAR();
  dft8<true>(v);
#pragma unroll
  for (int n = 0; n < 8; ++n) L[hi * 72 + n * 8 + lo] = v[n];
  WAVE_BAR();
#pragma unroll
  for (int k = 0; k < 8; ++k) v[k] = L[k * 72 + l];
  WAVE_BAR();
#pragma unroll
  for (int k = 1; k < 8; ++k) v[k] = cmulc(v[k], t1[k]);
  dft8<true>(v);
  STAMP(4);
  unsigned* lp = (unsigned*)L;
#pragma unroll
  for (int m = 0; m < 8; ++m) lp[l + 64 * m] = pack2(v[m].x, v[m].y);
  WAVE_BAR();
  u4 y0 = *(const u4*)(lp + l * 8), y1 = *(const u4*)(lp + l * 8 + 4);
  float a[8], c[8];
#pragma unroll
  for (int j = 0; j < 4; ++j) {
    unsigned ya = y0[j], yb = y1[j];
    float xa0 = (j & 1) ? bf2f_hi(ra[j >> 1]) : bf2f(ra[j >> 1] & 0xffffu);
    float xb0 = (j & 1) ? bf2f_hi(rb[j >> 1]) : bf2f(rb[j >> 1] & 0xffffu);
    float xa1 = (j & 1) ? bf2f_hi(ra[2 + (j >> 1)]) : bf2f(ra[2 + (j >> 1)] & 0xffffu);
    float xb1 = (j & 1) ? bf2f_hi(rb[2 + (j >> 1)]) : bf2f(rb[2 + (j >> 1)] & 0xffffu);
    a[j] = rbf(xa0 + bf2f(ya & 0xffffu));
    c[j] = rbf(xb0 + bf2f_hi(ya));
    a[j + 4] = rbf(xa1 + bf2f(yb & 0xffffu));
    c[j + 4] = rbf(xb1 + bf2f_hi(yb));
  }
  u4 ha, hb;
#pragma unroll
  for (int j = 0; j < 4; ++j) {
    ha[j] = (__float_as_uint(a[2 * j]) >> 16) | (__float_as_uint(a[2 * j + 1]) & 0xffff0000u);
    hb[j] = (__float_as_uint(c[2 * j]) >> 16) | (__float_as_uint(c[2 * j + 1]) & 0xffff0000u);
  }
  *(u4*)(h + (size_t)row * 512 + l * 8) = ha;
  *(u4*)(h + (size_t)(row + 1) * 512 + l * 8) = hb;
  ln_pair(a, c);
#pragma unroll
  for (int j = 0; j < 4; ++j) {
    ha[j] = (__float_as_uint(a[2 * j]) >> 16) | (__float_as_uint(a[2 * j + 1]) & 0xffff0000u);
    hb[j] = (__float_as_uint(c[2 * j]) >> 16) | (__float_as_uint(c[2 * j + 1]) & 0xffff0000u);
  }
  {
    int mt = row >> 7, rbk = (row >> 4) & 7, kb = l >> 2, ch = l & 3;
    size_t base = (((size_t)mt * 16 + kb) * 8 + rbk) * 64 + 16 * ch;
    *(u4*)(ln2 + (base + (row & 15)) * 8) = ha;
    *(u4*)(ln2 + (base + ((row + 1) & 15)) * 8) = hb;
  }
  STAMP(5);
  STAMP_FLUSH(2);
}

#define EXP_PD(x) (x)
#define GLDS(gp, lp) __builtin_amdgcn_global_load_lds((const unsigned*)(gp), (unsigned*)(lp), 16, 0, 0)
#define WAIT_V(n) asm volatile("s_waitcnt vmcnt(" #n ")" ::: "memory")
#define WAIT_L0() asm volatile("s_waitcnt lgkmcnt(0)" ::: "memory")
#define RAW_BAR() do { asm volatile("s_waitcnt lgkmcnt(0)" ::: "memory"); __builtin_amdgcn_s_barrier(); } while (0)
typedef short s4v __attribute__((ext_vector_type(4)));
#define LDS_TR(dst, addr, off) asm volatile("ds_read_b64_tr_b16 %0, %1 offset:%2" : "=v"(dst) : "v"(addr), "i"(off))
#define LDS_B128(dst, addr, off) asm volatile("ds_read_b128 %0, %1 offset:%2" : "=v"(dst) : "v"(addr), "i"(off))
#define WAIT_L(n) asm volatile("s_waitcnt lgkmcnt(" #n ")" ::: "memory")
#define CAT8(a, b) ((bf8){a[0], a[1], a[2], a[3], b[0], b[1], b[2], b[3]})
#define SCHED0() __builtin_amdgcn_sched_barrier(0)

__device__ __forceinline__ float gelu_t(float x) {
  float y2 = -2.3022082007793f * (x + 0.044715f * x * x * x);
  return x * __builtin_amdgcn_rcpf(1.0f + __builtin_amdgcn_exp2f(y2));
}

__global__ __launch_bounds__(512) void k4_gemm1(const us* __restrict__ ln2, const us* __restrict__ wg,
                                                const us* __restrict__ wu, const us* __restrict__ wd,
                                                us* __restrict__ act) {
  __shared__ __attribute__((aligned(16))) char sm[3 * 40960 + 8192];
  const int tid = threadIdx.x, wid = tid >> 6, lane = tid & 63;
  const int xcd = blockIdx.x & 7, idx = blockIdx.x >> 3;
  const int mt = 2 * xcd + (idx >> 4), ft = idx & 15;
  const int wm = wid >> 1, wn = wid & 1;
  STAMP_DECL; STAMP(0);
  const char* gA = (const char*)ln2 + (size_t)mt * 131072 + tid * 16;
  const char* gB[3];
#pragma unroll
  for (int i = 0; i < 3; ++i) {
    int q = i * 512 + tid;
    int mat = q >= 768 ? 1 : 0;
    int qq = q - mat * 768;
    int r = qq / 12, cp = qq - r * 12;
    int cch = cp ^ (((r >> 3) & 1) << 1);
    int col0 = 88 * ft + 8 * cch;
    col0 = col0 > 1368 ? 1368 : col0;
    gB[i] = (const char*)((mat ? wu : wg) + (size_t)r * 1376 + col0);
  }
  auto stage = [&](int s, int buf) {
    char* dst = sm + buf * 40960 + wid * 1024;
    GLDS(gA + s * 16384, dst);
    GLDS(gA + s * 16384 + 8192, dst + 8192);
    GLDS(gB[0] + (size_t)s * 64 * 2752, dst + 16384);
    GLDS(gB[1] + (size_t)s * 64 * 2752, dst + 16384 + 8192);
    GLDS(gB[2] + (size_t)s * 64 * 2752, dst + 16384 + 16384);
  };
  f4 ag[2][3], au[2][3];
#pragma unroll
  for (int i = 0; i < 2; ++i)
#pragma unroll
    for (int f = 0; f < 3; ++f) { ag[i][f] = (f4){0, 0, 0, 0}; au[i][f] = (f4){0, 0, 0, 0}; }
  STAMP(12);
  stage(0, 0);
  STAMP(13);
  __builtin_amdgcn_s_barrier();
  stage(1, 1);
  stage(2, 2);
  STAMP(14);
  const int g = lane >> 4, q4 = (lane & 15) >> 2, p4 = lane & 3;
  const unsigned smb = (unsigned)(size_t)sm;
  const unsigned tb = smb + 16384 + (8 * g + q4) * 192 + 8 * (p4 & 1) + 16 * (p4 >> 1) + 96 * wn;
  const int sgn = 32 * (g & 1);
  const unsigned tb0 = tb + (wn ? -sgn : sgn), tb1 = tb + (wn ? sgn : -sgn);
  const unsigned ab = smb + lane * 16 + wm * 2048;
  bf8 xb[2][2][2];
  s4v wl[2][2][2][3][2];
#define RD1(st, set, r) do {                                                                        \
    const unsigned bo = ((st) % 3) * 40960;                                                         \
    constexpr int kk_ = (r) / 14, e_ = (r) % 14;                                                    \
    if constexpr (e_ < 2) { LDS_B128(xb[set][kk_][e_], ab + bo, kk_ * 8192 + e_ * 1024); }          \
    else { constexpr int m_ = (e_ - 2) / 6, f_ = ((e_ - 2) % 6) / 2, t_ = (e_ - 2) % 2;             \
      LDS_TR(wl[set][kk_][m_][f_][t_], ((f_ & 1) ? tb1 : tb0) + bo, kk_ * 6144 + m_ * 12288 + f_ * 32 + t_ * 768); } \
  } while (0)
#define RD4(st, set, r) do { RD1(st, set, r); RD1(st, set, r + 1); RD1(st, set, r + 2); RD1(st, set, r + 3); } while (0)
  WAIT_V(10);
  RAW_BAR();
  STAMP(1);
  RD4(0, 0, 0); RD4(0, 0, 4); RD4(0, 0, 8); RD4(0, 0, 12); RD4(0, 0, 16); RD4(0, 0, 20); RD4(0, 0, 24);
  const char* pd = (const char*)wd + (size_t)idx * 44032 + tid * 16;
  char* dump = sm + 3 * 40960 + wid * 1024;
#pragma unroll
  for (int s = 0; s < 8; ++s) {
    const int cur = s & 1, nx = cur ^ 1;
    if (s < 6) WAIT_V(5); else if (s == 6) WAIT_V(6);
    RAW_BAR();
    SCHED0();
    STAMP(2 + s);
    char* dst = sm + (s % 3) * 40960 + wid * 1024;
#define MM(kk, f, i) do {                                                                                   \
      ag[i][f] = __builtin_amdgcn_mfma_f32_16x16x32_bf16(CAT8(wl[cur][kk][0][f][0], wl[cur][kk][0][f][1]),    \
                                                         xb[cur][kk][i], ag[i][f], 0, 0, 0);                \
      au[i][f] = __builtin_amdgcn_mfma_f32_16x16x32_bf16(CAT8(wl[cur][kk][1][f][0], wl[cur][kk][1][f][1]),    \
                                                         xb[cur][kk][i], au[i][f], 0, 0, 0); } while (0)
#define DMA(j) do {                                                                                  \
      if (s + 3 < 8) {                                                                               \
        if (j == 0) GLDS(gA + (s + 3) * 16384, dst);                                                 \
        if (j == 1) GLDS(gA + (s + 3) * 16384 + 8192, dst + 8192);                                   \
        if (j >= 2 && j < 5) GLDS(gB[j >= 2 ? j - 2 : 0] + (size_t)(s + 3) * 64 * 2752, dst + 16384 + (j - 2) * 8192); \
      } else if (s == 5) {                                                                           \
        if (j < 5) GLDS(pd + j * 8192, dump);                                                        \
        else GLDS(pd + (tid < 192 ? 5 * 8192 : 0), dump);                                            \
      } } while (0)
#define RDN(r) do { if (s + 1 < 8) { RD1(s + 1, nx, r); RD1(s + 1, nx, r + 1); } } while (0)
#define RDN3(r) do { if (s + 1 < 8) { RD1(s + 1, nx, r); RD1(s + 1, nx, r + 1); RD1(s + 1, nx, r + 2); } } while (0)
    MM(0, 0, 0); SCHED0(); DMA(0); RDN(0);   SCHED0();
    MM(0, 0, 1); SCHED0(); RDN3(2);          SCHED0();
    MM(0, 1, 0); SCHED0(); DMA(1); RDN(5);   SCHED0();
    MM(0, 1, 1); SCHED0(); RDN3(7);          SCHED0();
    MM(0, 2, 0); SCHED0(); DMA(2); RDN(10);  SCHED0();
    MM(0, 2, 1); SCHED0(); RDN3(12);         SCHED0();
    MM(1, 0, 0); SCHED0(); DMA(3); RDN(15);  SCHED0();
    MM(1, 0, 1); SCHED0(); RDN3(17);         SCHED0();
    MM(1, 1, 0); SCHED0(); DMA(4); RDN(20);  SCHED0();
    MM(1, 1, 1); SCHED0(); RDN3(22);         SCHED0();
    MM(1, 2, 0); SCHED0(); DMA(5); RDN3(25); SCHED0();
    MM(1, 2, 1); SCHED0();
  }
  STAMP(10);
#pragma unroll
  for (int i = 0; i < 2; ++i) {
    int row = mt * 128 + wm * 32 + i * 16 + (lane & 15);
    int mt2 = row >> 6, rbl = (row >> 4) & 3;
#pragma unroll
    for (int f = 0; f < 3; ++f) {
      int c = wn * 48 + f * 16 + 4 * (lane >> 4);
      int k = 88 * ft + c;
      float r[4];
#pragma unroll
      for (int j = 0; j < 4; ++j) {
        float gg = rbf(ag[i][f][j]), uu = rbf(au[i][f][j]);
        r[j] = rbf(gelu_t(gg)) * uu;
      }
      u2 o = (u2){pack2(r[0], r[1]), pack2(r[2], r[3])};
      size_t off = ((((size_t)mt2 * 43 + (k >> 5)) * 4 + rbl) * 64 + (lane & 15) + 16 * ((k & 31) >> 3)) * 8 + (k & 7);
      if (c < 88 && k < 1376) *(u2*)(act + off) = o;
    }
  }
  WAIT_V(0);
  STAMP(11);
  STAMP_FLUSH(3);
}

__global__ __launch_bounds__(512) void k5_gemm2(const us* __restrict__ act, const us* __restrict__ wd,
                                                const us* __restrict__ h, us* __restrict__ out) {
  __shared__ __attribute__((aligned(16))) char sm[131072];
  const int tid = threadIdx.x, wid = tid >> 6, lane = tid & 63;
  const int xcd = blockIdx.x & 7, idx = blockIdx.x >> 3;
  const int mt2 = xcd * 4 + (idx >> 3), nt = idx & 7;
  STAMP_DECL; STAMP(0);
  char* ring = sm + wid * 16384;
  const char* gA = (const char*)act + ((size_t)mt2 * 43 * 4) * 1024 + lane * 16;
  const char* gW[4];
#pragma unroll
  for (int i = 0; i < 4; ++i) {
    int rho = 8 * i + (lane >> 3);
    int sw = ((rho >> 1) & 1) | (((rho >> 3) & 1) << 1);
    int ch = (lane & 7) ^ (sw << 1);
    gW[i] = (const char*)(wd + (size_t)rho * 512 + nt * 64 + ch * 8);
  }
  auto stage = [&](int kb, int buf) {
    char* dst = ring + buf * 8192;
#pragma unroll
    for (int i = 0; i < 4; ++i) GLDS(gA + (size_t)kb * 4096 + i * 1024, dst + i * 1024);
#pragma unroll
    for (int i = 0; i < 4; ++i) GLDS(gW[i] + (size_t)kb * 32 * 1024, dst + 4096 + i * 1024);
  };
  f4 acc[4][4];
#pragma unroll
  for (int i = 0; i < 4; ++i)
#pragma unroll
    for (int j = 0; j < 4; ++j) acc[i][j] = (f4){0, 0, 0, 0};
  const int g = lane >> 4, q4 = (lane & 15) >> 2, p4 = lane & 3;
  const int sw = ((q4 >> 1) & 1) | ((g & 1) << 1);
  const unsigned ringb = (unsigned)(size_t)sm + wid * 16384;
  const unsigned trb = ringb + 4096 + (8 * g + q4) * 128 + 8 * (p4 & 1) + 16 * (p4 >> 1);
  const unsigned tr0 = trb + 32 * (0 ^ sw), tr1 = trb + 32 * (1 ^ sw), tr2 = trb + 32 * (2 ^ sw), tr3 = trb + 32 * (3 ^ sw);
  const unsigned ab = ringb + lane * 16;
  const int nst = wid < 3 ? 6 : 5;
  stage(wid, 0);
  stage(wid + 8, 1);
#pragma unroll
  for (int i = 0; i < 6; ++i) {
    if (i < nst) {
      if (i + 1 < nst) WAIT_V(8); else WAIT_V(0);
      bf8 xa[4];
      s4v wl[4][2];
      LDS_B128(xa[0], ab, (i & 1) * 8192 + 0);
      LDS_B128(xa[1], ab, (i & 1) * 8192 + 1024);
      LDS_B128(xa[2], ab, (i & 1) * 8192 + 2048);
      LDS_B128(xa[3], ab, (i & 1) * 8192 + 3072);
      LDS_TR(wl[0][0], tr0, (i & 1) * 8192); LDS_TR(wl[0][1], tr0, (i & 1) * 8192 + 512);
      LDS_TR(wl[1][0], tr1, (i & 1) * 8192); LDS_TR(wl[1][1], tr1, (i & 1) * 8192 + 512);
      LDS_TR(wl[2][0], tr2, (i & 1) * 8192); LDS_TR(wl[2][1], tr2, (i & 1) * 8192 + 512);
      LDS_TR(wl[3][0], tr3, (i & 1) * 8192); LDS_TR(wl[3][1], tr3, (i & 1) * 8192 + 512);
      WAIT_L(0);
      SCHED0();
      if (i + 2 < nst) stage(wid + 8 * (i + 2), i & 1);
#pragma unroll
      for (int nb = 0; nb < 4; ++nb) {
        bf8 wb = CAT8(wl[nb][0], wl[nb][1]);
#pragma unroll
        for (int rb = 0; rb < 4; ++rb)
          acc[nb][rb] = __builtin_amdgcn_mfma_f32_16x16x32_bf16(wb, xa[rb], acc[nb][rb], 0, 0, 0);
      }
      SCHED0();
    }
  }
  STAMP(1);
  f4* red = (f4*)sm;
#pragma unroll
  for (int nb = 0; nb < 4; ++nb)
#pragma unroll
    for (int rb = 0; rb < 4; ++rb) red[(wid * 16 + nb * 4 + rb) * 64 + lane] = acc[nb][rb];
  __syncthreads();
  STAMP(2);
  const int rb = wid >> 1;
#pragma unroll
  for (int q = 0; q < 2; ++q) {
    int nb = 2 * (wid & 1) + q;
    f4 s = red[(nb * 4 + rb) * 64 + lane];
#pragma unroll
    for (int src = 1; src < 8; ++src) s += red[(src * 16 + nb * 4 + rb) * 64 + lane];
    int token = mt2 * 64 + rb * 16 + (lane & 15);
    int col = nt * 64 + nb * 16 + 4 * (lane >> 4);
    u2 hv = *(const u2*)(h + (size_t)token * 512 + col);
    float o0 = bf2f(hv[0] & 0xffffu) + rbf(s[0]);
    float o1 = bf2f_hi(hv[0]) + rbf(s[1]);
    float o2 = bf2f(hv[1] & 0xffffu) + rbf(s[2]);
    float o3 = bf2f_hi(hv[1]) + rbf(s[3]);
    *(u2*)(out + (size_t)token * 512 + col) = (u2){pack2(o0, o1), pack2(o2, o3)};
  }
  STAMP(3);
  STAMP_FLUSH(4);
}

extern "C" void kernel_launch(void* const* d_in, const int* in_sizes, int n_in, void* d_out, int out_size,
                              void* d_ws, size_t ws_size, hipStream_t stream) {
  const us* x = (const us*)d_in[0];
  const us* wr = (const us*)d_in[1];
  const us* wi = (const us*)d_in[2];
  const us* wg = (const us*)d_in[4];
  const us* wu = (const us*)d_in[5];
  const us* wd = (const us*)d_in[6];
  char* ws = (char*)d_ws;
  f2* X1 = (f2*)(ws);
  f2* X2 = (f2*)(ws + (5u << 20));
  us* h = (us*)(ws + (10u << 20));
  us* ln2 = (us*)(ws + (12u << 20));
  us* act = (us*)(ws + (14u << 20));
  unsigned* Wt = (unsigned*)(ws + (20u << 20));
  k1_lnfft<<<512, 256, 0, stream>>>(x, X1, wr, wi, Wt);
  k2_col<<<264 + 128, 256, 0, stream>>>(X1, X2, Wt, wg, wu);
  k3_ifft<<<256 + 128, 256, 0, stream>>>(X2, x, h, ln2, wg, wu);
  k4_gemm1<<<256, 512, 0, stream>>>(ln2, wg, wu, wd, act);
  k5_gemm2<<<256, 512, 0, stream>>>(act, wd, h, (us*)d_out);
}
```

```cpp
#include <hip/hip_runtime.h>
#include <hip/hip_bf16.h>
#include <stdint.h>

typedef float f2 __attribute__((ext_vector_type(2)));
typedef float f4 __attribute__((ext_vector_type(4)));
typedef unsigned u4 __attribute__((ext_vector_type(4)));
typedef unsigned u2 __attribute__((ext_vector_type(2)));
typedef short bf8 __attribute__((ext_vector_type(8)));
typedef unsigned short us;

#define WAVE_BAR() __builtin_amdgcn_wave_barrier()
#define ST16(ptr, val) asm volatile("global_store_dwordx4 %0, %1, off sc1\n\ts_nop 1" :: "v"(ptr), "v"(val) : "memory")
#define ST8(ptr, val) asm volatile("global_store_dwordx2 %0, %1, off sc1\n\ts_nop 1" :: "v"(ptr), "v"(val) : "memory")
#define STAMP_DECL
#define STAMP(i)
#define STAMPW(i)
#define STAMP_FLUSH(kid)

__device__ __forceinline__ float bf2f(unsigned h) { return __uint_as_float(h << 16); }
__device__ __forceinline__ float bf2f_hi(unsigned h) { return __uint_as_float(h & 0xffff0000u); }
__device__ __forceinline__ unsigned f2bf(float f) {
  __bf16 b = (__bf16)f;
  return (unsigned)__builtin_bit_cast(unsigned short, b);
}
__device__ __forceinline__ float rbf(float f) { return bf2f(f2bf(f)); }
typedef __bf16 bf2v __attribute__((ext_vector_type(2)));
__device__ __forceinline__ unsigned pack2(float lo, float hi) {
  bf2v t = __builtin_convertvector((f2){lo, hi}, bf2v);
  return __builtin_bit_cast(unsigned, t);
}

__device__ __forceinline__ f2 cmul(f2 a, f2 b) { return (f2){a.x * b.x - a.y * b.y, a.x * b.y + a.y * b.x}; }
__device__ __forceinline__ f2 cmulc(f2 a, f2 b) { return (f2){a.x * b.x + a.y * b.y, a.y * b.x - a.x * b.y}; }
template <bool INV> __device__ __forceinline__ f2 rot(f2 a) { return INV ? (f2){-a.y, a.x} : (f2){a.y, -a.x}; }

template <bool INV> __device__ __forceinline__ void dft8(f2 (&v)[8]) {
  const float r = 0.70710678118654752f;
  f2 a0 = v[0] + v[4], a1 = v[0] - v[4], a2 = v[2] + v[6], a3 = rot<INV>(v[2] - v[6]);
  f2 a4 = v[1] + v[5], a5 = v[1] - v[5], a6 = v[3] + v[7], a7 = rot<INV>(v[3] - v[7]);
  f2 e0 = a0 + a2, e2 = a0 - a2, e1 = a1 + a3, e3 = a1 - a3;
  f2 o0 = a4 + a6, o2 = a4 - a6, o1 = a5 + a7, o3 = a5 - a7;
  o1 = (o1 + rot<INV>(o1)) * r;
  o2 = rot<INV>(o2);
  o3 = (rot<INV>(o3) - o3) * r;
  v[0] = e0 + o0; v[4] = e0 - o0;
  v[1] = e1 + o1; v[5] = e1 - o1;
  v[2] = e2 + o2; v[6] = e2 - o2;
  v[3] = e3 + o3; v[7] = e3 - o3;
}
template <bool INV> __device__ __forceinline__ void dft4(f2& x0, f2& x1, f2& x2, f2& x3) {
  f2 a0 = x0 + x2, a1 = x0 - x2, a2 = x1 + x3, a3 = rot<INV>(x1 - x3);
  x0 = a0 + a2; x2 = a0 - a2; x1 = a1 + a3; x3 = a1 - a3;
}
__device__ __forceinline__ void make_tw(f2 (&t)[8], int num, float c2) {
  float s, c;
  sincospif(-(float)num * c2, &s, &c);
  t[1] = (f2){c, s};
  t[2] = cmul(t[1], t[1]);
  t[3] = cmul(t[2], t[1]);
  t[4] = cmul(t[2], t[2]);
  t[5] = cmul(t[4], t[1]);
  t[6] = cmul(t[3], t[3]);
  t[7] = cmul(t[4], t[3]);
}

template <int ctrl, int rm> __device__ __forceinline__ float dpp_add(float v) {
  int t = __builtin_amdgcn_update_dpp(0, __float_as_int(v), ctrl, rm, 0xf, false);
  return v + __int_as_float(t);
}
__device__ __forceinline__ float wave_sum(float v) {
  v = dpp_add<0x111, 0xf>(v);
  v = dpp_add<0x112, 0xf>(v);
  v = dpp_add<0x114, 0xf>(v);
  v = dpp_add<0x118, 0xf>(v);
  v = dpp_add<0x142, 0xa>(v);
  v = dpp_add<0x143, 0xc>(v);
  return __int_as_float(__builtin_amdgcn_readlane(__float_as_int(v), 63));
}

__device__ __forceinline__ void ln_pair(float (&a)[8], float (&b)[8]) {
  float sa = 0.f, sb = 0.f;
#pragma unroll
  for (int j = 0; j < 8; ++j) { sa += a[j]; sb += b[j]; }
  sa = wave_sum(sa); sb = wave_sum(sb);
  float ma = sa * (1.0f / 512.0f), mb = sb * (1.0f / 512.0f);
  float va = 0.f, vb = 0.f;
#pragma unroll
  for (int j = 0; j < 8; ++j) { float d = a[j] - ma; va += d * d; float e = b[j] - mb; vb += e * e; }
  va = wave_sum(va) * (1.0f / 512.0f); vb = wave_sum(vb) * (1.0f / 512.0f);
  float mra = rbf(ma), mrb = rbf(mb);
  float da = rbf(sqrtf(rbf(rbf(va) + 1e-5f))), db = rbf(sqrtf(rbf(rbf(vb) + 1e-5f)));
  const float ra_ = 1.0f / da, rb_ = 1.0f / db;
#pragma unroll
  for (int j = 0; j < 8; ++j) {
    float na = rbf(a[j] - mra), nb = rbf(b[j] - mrb);
    float qa = na * ra_, qb = nb * rb_;
    qa = __builtin_fmaf(__builtin_fmaf(-qa, da, na), ra_, qa);
    qb = __builtin_fmaf(__builtin_fmaf(-qb, db, nb), rb_, qb);
    a[j] = rbf(qa); b[j] = rbf(qb);
  }
}

__device__ __forceinline__ int p_of_q(int q) {
  int T = q >> 3, e = q & 7;
  int k0 = T >> 5, k1 = (T >> 2) & 7, k2 = 2 * (T & 3) + (e >> 2), k3 = e & 3;
  return k0 + 8 * k1 + 64 * k2 + 512 * k3;
}

#define PFD 24
#define PF_DELAY() do { for (int i_ = 0; i_ < PFD; ++i_) __builtin_amdgcn_s_sleep(2); } while (0)
#define WSTR 584

__global__ __launch_bounds__(256) void k1_lnfft(const us* __restrict__ x, f2* __restrict__ X1,
                                                const us* __restrict__ wr, const us* __restrict__ wi,
                                                unsigned* __restrict__ Wt) {
  __shared__ __attribute__((aligned(16))) f2 lds[4 * WSTR];
  const int tid = threadIdx.x;
  if (blockIdx.x >= 256) {
    unsigned* tile = (unsigned*)lds;
    const int xg = blockIdx.x & 7, qi = (blockIdx.x - 256) >> 3;
    const int c = tid & 31;
    for (int i_ = 0; i_ < 8; ++i_) __builtin_amdgcn_s_sleep(2);
#pragma unroll
    for (int i = 0; i < 8; ++i) {
      int qq = (tid >> 5) + 8 * i;
      int pp = p_of_q(64 * qi + qq);
      unsigned r_ = wr[pp * 257 + 32 * xg + c], i_ = wi[pp * 257 + 32 * xg + c];
      tile[c * 65 + qq] = r_ | (i_ << 16);
    }
    if (xg == 0 && tid < 64) {
      int pp = p_of_q(64 * qi + tid);
      Wt[(size_t)256 * 2048 + 64 * qi + tid] = (unsigned)wr[pp * 257 + 256] | ((unsigned)wi[pp * 257 + 256] << 16);
    }
    __syncthreads();
#pragma unroll
    for (int i = 0; i < 2; ++i) {
      int cc = (tid >> 4) + 16 * i, q0 = 4 * (tid & 15);
      u4 o = (u4){tile[cc * 65 + q0], tile[cc * 65 + q0 + 1], tile[cc * 65 + q0 + 2], tile[cc * 65 + q0 + 3]};
      ST16((u4*)(Wt + (size_t)(32 * xg + cc) * 2048 + 64 * qi + q0), o);
    }
    return;
  }
  const int b = (blockIdx.x & 7) * 32 + (blockIdx.x >> 3);
  const int w = tid >> 6, l = tid & 63;
  STAMP_DECL; STAMP(0);
  const int row = b * 8 + w * 2;
  u4 ra = *(const u4*)(x + (size_t)row * 512 + l * 8);
  u4 rb = *(const u4*)(x + (size_t)(row + 1) * 512 + l * 8);
  f2 t1[8], t2[8];
  make_tw(t1, l, 2.0f / 512.0f);
  make_tw(t2, l & 7, 2.0f / 64.0f);
  STAMP(1); STAMPW(2);
  float a[8], c[8];
#pragma unroll
  for (int j = 0; j < 4; ++j) {
    a[2 * j] = bf2f(ra[j] & 0xffffu); a[2 * j + 1] = bf2f_hi(ra[j]);
    c[2 * j] = bf2f(rb[j] & 0xffffu); c[2 * j + 1] = bf2f_hi(rb[j]);
  }
  ln_pair(a, c);
  STAMP(3);
  f2* L = lds + w * WSTR;
  unsigned* lp = (unsigned*)L;
  u4 p0, p1;
#pragma unroll
  for (int j = 0; j < 4; ++j) {
    p0[j] = (__float_as_uint(a[j]) >> 16) | (__float_as_uint(c[j]) & 0xffff0000u);
    p1[j] = (__float_as_uint(a[j + 4]) >> 16) | (__float_as_uint(c[j + 4]) & 0xffff0000u);
  }
  *(u4*)(lp + l * 8) = p0;
  *(u4*)(lp + l * 8 + 4) = p1;
  WAVE_BAR();
  f2 v[8];
#pragma unroll
  for (int m = 0; m < 8; ++m) { unsigned q = lp[l + 64 * m]; v[m] = (f2){bf2f(q & 0xffffu), bf2f_hi(q)}; }
  WAVE_BAR();
  dft8<false>(v);
#pragma unroll
  for (int k = 1; k < 8; ++k) v[k] = cmul(v[k], t1[k]);
#pragma unroll
  for (int k = 0; k < 8; ++k) L[k * 72 + l] = v[k];
  WAVE_BAR();
  const int hi = l >> 3, lo = l & 7;
#pragma unroll
  for (int n = 0; n < 8; ++n) v[n] = L[hi * 72 + n * 8 + lo];
  WAVE_BAR();
  dft8<false>(v);
#pragma unroll
  for (int k = 1; k < 8; ++k) v[k] = cmul(v[k], t2[k]);
#pragma unroll
  for (int k = 0; k < 8; ++k) L[hi * 72 + k * 9 + lo] = v[k];
  WAVE_BAR();
#pragma unroll
  for (int n = 0; n < 8; ++n) v[n] = L[hi * 72 + lo * 9 + n];
  WAVE_BAR();
  dft8<false>(v);
#pragma unroll
  for (int k = 0; k < 8; ++k) L[hi + 9 * lo + 72 * k] = v[k];
  STAMP(4);
  __syncthreads();
  STAMP(5);
  const int p = tid & 3;
  const f2* Lp = lds + p * WSTR;
#pragma unroll
  for (int i = 0; i < 4; ++i) {
    int k = (tid >> 2) + 64 * i;
    int kk = (512 - k) & 511;
    f2 z = Lp[k + (k >> 3)];
    f2 y = Lp[kk + (kk >> 3)];
    f4 o = (f4){0.5f * (z.x + y.x), 0.5f * (z.y - y.y), 0.5f * (z.y + y.y), 0.5f * (y.x - z.x)};
    ST16((f4*)(X1 + (size_t)k * 2048 + b * 8 + 2 * p), o);
  }
  if (tid < 4) {
    f2 z = Lp[256 + 32];
    *(f4*)(X1 + (size_t)256 * 2048 + b * 8 + 2 * p) = (f4){z.x, 0.f, z.y, 0.f};
  }
  STAMP(6);
  STAMP_FLUSH(0);
}

__global__ __launch_bounds__(256) void k2_col(const f2* __restrict__ X1, f2* __restrict__ X2,
                                              const unsigned* __restrict__ Wt,
                                              const us* __restrict__ wg, const us* __restrict__ wu) {
  __shared__ __attribute__((aligned(16))) f2 lds[8 * 288];
  if (blockIdx.x >= 264) {
    const char* pg = (const char*)wg + ((blockIdx.x - 264) >> 3) * 88064 + threadIdx.x * 16;
    u4 acc = {0, 0, 0, 0};
    PF_DELAY();
#pragma unroll
    for (int i = 0; i < 21; ++i) acc ^= *(const u4*)(pg + i * 4096);
    if (threadIdx.x < 128) acc ^= *(const u4*)(pg + 21 * 4096);
    asm volatile("" ::"v"(acc));
    return;
  }
  if (blockIdx.x > 256) return;
  const int c = blockIdx.x < 256 ? (blockIdx.x & 7) * 32 + (blockIdx.x >> 3) : 256;
  const int t = threadIdx.x;
  const f2* col = X1 + (size_t)c * 2048;
  STAMP_DECL; STAMP(0);
  f2 v[8];
#pragma unroll
  for (int m = 0; m < 8; ++m) v[m] = col[t + 256 * m];
  u4 w0 = *(const u4*)(Wt + (size_t)c * 2048 + t * 8);
  u4 w1 = *(const u4*)(Wt + (size_t)c * 2048 + t * 8 + 4);
  f2 t1[8], t2[8], t3[8];
  make_tw(t1, t, 2.0f / 2048.0f);
  make_tw(t2, t & 31, 2.0f / 256.0f);
  make_tw(t3, t & 3, 2.0f / 32.0f);
  STAMP(1);
  const int g = t >> 5, lo5 = t & 31;
  f2* R = lds + g * 288;
  dft8<false>(v);
#pragma unroll
  for (int k = 1; k < 8; ++k) v[k] = cmul(v[k], t1[k]);
#pragma unroll
  for (int k = 0; k < 8; ++k) lds[k * 288 + t] = v[k];
  STAMP(2);
  __syncthreads();
#pragma unroll
  for (int n = 0; n < 8; ++n) v[n] = R[n * 32 + lo5];
  WAVE_BAR();
  dft8<false>(v);
#pragma unroll
  for (int k = 1; k < 8; ++k) v[k] = cmul(v[k], t2[k]);
#pragma unroll
  for (int k = 0; k < 8; ++k) R[k * 36 + lo5] = v[k];
  WAVE_BAR();
  const int m3 = (t >> 2) & 7, q3 = t & 3;
#pragma unroll
  for (int n = 0; n < 8; ++n) v[n] = R[m3 * 36 + 4 * n + q3];
  WAVE_BAR();
  dft8<false>(v);
#pragma unroll
  for (int k = 1; k < 8; ++k) v[k] = cmul(v[k], t3[k]);
#pragma unroll
  for (int k = 0; k < 8; ++k) R[m3 * 34 + 4 * k + q3] = v[k];
  WAVE_BAR();
#pragma unroll
  for (int e = 0; e < 8; ++e) v[e] = R[m3 * 34 + 8 * q3 + e];
  WAVE_BAR();
  dft4<false>(v[0], v[1], v[2], v[3]);
  dft4<false>(v[4], v[5], v[6], v[7]);
  STAMP(3);
  {
    const float sc = 1.0f / 1048576.0f;
#pragma unroll
    for (int e = 0; e < 4; ++e) {
      v[e] = cmul(v[e], (f2){bf2f(w0[e] & 0xffffu) * sc, bf2f_hi(w0[e]) * sc});
      v[e + 4] = cmul(v[e + 4], (f2){bf2f(w1[e] & 0xffffu) * sc, bf2f_hi(w1[e]) * sc});
    }
  }
  STAMP(4);
  dft4<true>(v[0], v[1], v[2], v[3]);
  dft4<true>(v[4], v[5], v[6], v[7]);
#pragma unroll
  for (int e = 0; e < 8; ++e) R[m3 * 34 + 8 * q3 + e] = v[e];
  WAVE_BAR();
#pragma unroll
  for (int k = 0; k < 8; ++k) v[k] = R[m3 * 34 + 4 * k + q3];
  WAVE_BAR();
#pragma unroll
  for (int k = 1; k < 8; ++k) v[k] = cmulc(v[k], t3[k]);
  dft8<true>(v);
#pragma unroll
  for (int n = 0; n < 8; ++n) R[m3 * 36 + 4 * n + q3] = v[n];
  WAVE_BAR();
#pragma unroll
  for (int k = 0; k < 8; ++k) v[k] = R[k * 36 + lo5];
  WAVE_BAR();
#pragma unroll
  for (int k = 1; k < 8; ++k) v[k] = cmulc(v[k], t2[k]);
  dft8<true>(v);
#pragma unroll
  for (int n = 0; n < 8; ++n) R[n * 32 + lo5] = v[n];
  __syncthreads();
#pragma unroll
  for (int k = 0; k < 8; ++k) v[k] = lds[k * 288 + t];
#pragma unroll
  for (int k = 1; k < 8; ++k) v[k] = cmulc(v[k], t1[k]);
  dft8<true>(v);
  STAMP(5);
  f2* ocol = X2 + (size_t)c * 2048;
#pragma unroll
  for (int m = 0; m < 8; ++m) ST8(ocol + t + 256 * m, v[m]);
  STAMP(6);
  STAMP_FLUSH(1);
}

__global__ __launch_bounds__(256) void k3_ifft(const f2* __restrict__ X2, const us* __restrict__ x,
                                               us* __restrict__ h, us* __restrict__ ln2,
                                               const us* __restrict__ wg, const us* __restrict__ wu) {
  __shared__ __attribute__((aligned(16))) f2 lds[4 * WSTR];
  const int tid = threadIdx.x;
  if (blockIdx.x >= 256) {
    const char* pu = (const char*)wu + ((blockIdx.x - 256) >> 3) * 88064 + threadIdx.x * 16;
    u4 acc = {0, 0, 0, 0};
    PF_DELAY();
#pragma unroll
    for (int i = 0; i < 21; ++i) acc ^= *(const u4*)(pu + i * 4096);
    if (threadIdx.x < 128) acc ^= *(const u4*)(pu + 21 * 4096);
    asm volatile("" ::"v"(acc));
    return;
  }
  const int b = (blockIdx.x & 7) * 32 + (blockIdx.x >> 3);
  const int w = tid >> 6, l = tid & 63;
  const int p = tid & 3;
  STAMP_DECL; STAMP(0);
  f4 o[4];
#pragma unroll
  for (int i = 0; i < 4; ++i) {
    int k = (tid >> 2) + 64 * i;
    o[i] = *(const f4*)(X2 + (size_t)k * 2048 + b * 8 + 2 * p);
  }
  f4 ony = *(const f4*)(X2 + (size_t)256 * 2048 + b * 8 + 2 * p);
  const int row = b * 8 + w * 2;
  u4 ra = *(const u4*)(x + (size_t)row * 512 + l * 8);
  u4 rb = *(const u4*)(x + (size_t)(row + 1) * 512 + l * 8);
  f2 t1[8], t2[8];
  make_tw(t1, l, 2.0f / 512.0f);
  make_tw(t2, l & 7, 2.0f / 64.0f);
  STAMP(1); STAMPW(2);
  f2* Lp = lds + p * WSTR;
#pragma unroll
  for (int i = 0; i < 4; ++i) {
    int k = (tid >> 2) + 64 * i;
    int kk = (512 - k) & 511;
    f4 z = o[i];
    if (k == 0) {
      Lp[0] = (f2){z.x, z.z};
      Lp[256 + 32] = (f2){ony.x, ony.z};
    } else {
      Lp[k + (k >> 3)] = (f2){z.x - z.w, z.y + z.z};
      Lp[kk + (kk >> 3)] = (f2){z.x + z.w, z.z - z.y};
    }
  }
  __syncthreads();
  STAMP(3);
  f2* L = lds + w * WSTR;
  const int hi = l >> 3, lo = l & 7;
  f2 v[8];
#pragma unroll
  for (int k = 0; k < 8; ++k) v[k] = L[hi + 9 * lo + 72 * k];
  WAVE_BAR();
  dft8<true>(v);
#pragma unroll
  for (int n = 1; n < 8; ++n) v[n] = cmulc(v[n], t2[n]);
#pragma unroll
  for (int n = 0; n < 8; ++n) L[hi * 72 + lo * 9 + n] = v[n];
  WAVE_BAR();
#pragma unroll
  for (int k = 0; k < 8; ++k) v[k] = L[hi * 72 + k * 9 + lo];
  WAVE_BAR();
  dft8<true>(v);
#pragma unroll
  for (int n = 0; n < 8; ++n) L[hi * 72 + n * 8 + lo] = v[n];
  WAVE_BAR();
#pragma unroll
  for (int k = 0; k < 8; ++k) v[k] = L[k * 72 + l];
  WAVE_BAR();
#pragma unroll
  for (int k = 1; k < 8; ++k) v[k] = cmulc(v[k], t1[k]);
  dft8<true>(v);
  STAMP(4);
  unsigned* lp = (unsigned*)L;
#pragma unroll
  for (int m = 0; m < 8; ++m) lp[l + 64 * m] = pack2(v[m].x, v[m].y);
  WAVE_BAR();
  u4 y0 = *(const u4*)(lp + l * 8), y1 = *(const u4*)(lp + l * 8 + 4);
  float a[8], c[8];
#pragma unroll
  for (int j = 0; j < 4; ++j) {
    unsigned ya = y0[j], yb = y1[j];
    float xa0 = (j & 1) ? bf2f_hi(ra[j >> 1]) : bf2f(ra[j >> 1] & 0xffffu);
    float xb0 = (j & 1) ? bf2f_hi(rb[j >> 1]) : bf2f(rb[j >> 1] & 0xffffu);
    float xa1 = (j & 1) ? bf2f_hi(ra[2 + (j >> 1)]) : bf2f(ra[2 + (j >> 1)] & 0xffffu);
    float xb1 = (j & 1) ? bf2f_hi(rb[2 + (j >> 1)]) : bf2f(rb[2 + (j >> 1)] & 0xffffu);
    a[j] = rbf(xa0 + bf2f(ya & 0xffffu));
    c[j] = rbf(xb0 + bf2f_hi(ya));
    a[j + 4] = rbf(xa1 + bf2f(yb & 0xffffu));
    c[j + 4] = rbf(xb1 + bf2f_hi(yb));
  }
  u4 ha, hb;
#pragma unroll
  for (int j = 0; j < 4; ++j) {
    ha[j] = (__float_as_uint(a[2 * j]) >> 16) | (__float_as_uint(a[2 * j + 1]) & 0xffff0000u);
    hb[j] = (__float_as_uint(c[2 * j]) >> 16) | (__float_as_uint(c[2 * j + 1]) & 0xffff0000u);
  }
  ST16((u4*)(h + (size_t)row * 512 + l * 8), ha);
  ST16((u4*)(h + (size_t)(row + 1) * 512 + l * 8), hb);
  ln_pair(a, c);
#pragma unroll
  for (int j = 0; j < 4; ++j) {
    ha[j] = (__float_as_uint(a[2 * j]) >> 16) | (__float_as_uint(a[2 * j + 1]) & 0xffff0000u);
    hb[j] = (__float_as_uint(c[2 * j]) >> 16) | (__float_as_uint(c[2 * j + 1]) & 0xffff0000u);
  }
  {
    int mt = row >> 7, rbk = (row >> 4) & 7, kb = l >> 2, ch = l & 3;
    size_t base = (((size_t)mt * 16 + kb) * 8 + rbk) * 64 + 16 * ch;
    *(u4*)(ln2 + (base + (row & 15)) * 8) = ha;
    *(u4*)(ln2 + (base + ((row + 1) & 15)) * 8) = hb;
  }
  STAMP(5);
  STAMP_FLUSH(2);
}

#define EXP_PD(x) (x)
#define GLDS(gp, lp) __builtin_amdgcn_global_load_lds((const unsigned*)(gp), (unsigned*)(lp), 16, 0, 0)
#define WAIT_V(n) asm volatile("s_waitcnt vmcnt(" #n ")" ::: "memory")
#define WAIT_L0() asm volatile("s_waitcnt lgkmcnt(0)" ::: "memory")
#define RAW_BAR() do { asm volatile("s_waitcnt lgkmcnt(0)" ::: "memory"); __builtin_amdgcn_s_barrier(); } while (0)
typedef short s4v __attribute__((ext_vector_type(4)));
#define LDS_TR(dst, addr, off) asm volatile("ds_read_b64_tr_b16 %0, %1 offset:%2" : "=v"(dst) : "v"(addr), "i"(off))
#define LDS_B128(dst, addr, off) asm volatile("ds_read_b128 %0, %1 offset:%2" : "=v"(dst) : "v"(addr), "i"(off))
#define WAIT_L(n) asm volatile("s_waitcnt lgkmcnt(" #n ")" ::: "memory")
#define CAT8(a, b) ((bf8){a[0], a[1], a[2], a[3], b[0], b[1], b[2], b[3]})
#define SCHED0() __builtin_amdgcn_sched_barrier(0)

__device__ __forceinline__ float gelu_t(float x) {
  float y2 = -2.3022082007793f * (x + 0.044715f * x * x * x);
  return x * __builtin_amdgcn_rcpf(1.0f + __builtin_amdgcn_exp2f(y2));
}

__global__ __launch_bounds__(512) void k4_gemm1(const us* __restrict__ ln2, const us* __restrict__ wg,
                                                const us* __restrict__ wu, const us* __restrict__ wd,
                                                us* __restrict__ act) {
  __shared__ __attribute__((aligned(16))) char sm[3 * 40960 + 8192];
  const int tid = threadIdx.x, wid = tid >> 6, lane = tid & 63;
  const int xcd = blockIdx.x & 7, idx = blockIdx.x >> 3;
  const int mt = 2 * xcd + (idx >> 4), ft = idx & 15;
  const int wm = wid >> 1, wn = wid & 1;
  STAMP_DECL; STAMP(0);
  const char* gA = (const char*)ln2 + (size_t)mt * 131072 + tid * 16;
  const char* gB[3];
#pragma unroll
  for (int i = 0; i < 3; ++i) {
    int q = i * 512 + tid;
    int mat = q >= 768 ? 1 : 0;
    int qq = q - mat * 768;
    int r = qq / 12, cp = qq - r * 12;
    int cch = cp ^ (((r >> 3) & 1) << 1);
    int col0 = 88 * ft + 8 * cch;
    col0 = col0 > 1368 ? 1368 : col0;
    gB[i] = (const char*)((mat ? wu : wg) + (size_t)r * 1376 + col0);
  }
  auto stage = [&](int s, int buf) {
    char* dst = sm + buf * 40960 + wid * 1024;
    GLDS(gA + s * 16384, dst);
    GLDS(gA + s * 16384 + 8192, dst + 8192);
    GLDS(gB[0] + (size_t)s * 64 * 2752, dst + 16384);
    GLDS(gB[1] + (size_t)s * 64 * 2752, dst + 16384 + 8192);
    GLDS(gB[2] + (size_t)s * 64 * 2752, dst + 16384 + 16384);
  };
  f4 ag[2][3], au[2][3];
#pragma unroll
  for (int i = 0; i < 2; ++i)
#pragma unroll
    for (int f = 0; f < 3; ++f) { ag[i][f] = (f4){0, 0, 0, 0}; au[i][f] = (f4){0, 0, 0, 0}; }
  STAMP(12);
  stage(0, 0);
  STAMP(13);
  __builtin_amdgcn_s_barrier();
  stage(1, 1);
  stage(2, 2);
  STAMP(14);
  const int g = lane >> 4, q4 = (lane & 15) >> 2, p4 = lane & 3;
  const unsigned smb = (unsigned)(size_t)sm;
  const unsigned tb = smb + 16384 + (8 * g + q4) * 192 + 8 * (p4 & 1) + 16 * (p4 >> 1) + 96 * wn;
  const int sgn = 32 * (g & 1);
  const unsigned tb0 = tb + (wn ? -sgn : sgn), tb1 = tb + (wn ? sgn : -sgn);
  const unsigned ab = smb + lane * 16 + wm * 2048;
  bf8 xb[2][2][2];
  s4v wl[2][2][2][3][2];
#define RD1(st, set, r) do {                                                                        \
    const unsigned bo = ((st) % 3) * 40960;                                                         \
    constexpr int kk_ = (r) / 14, e_ = (r) % 14;                                                    \
    if constexpr (e_ < 2) { LDS_B128(xb[set][kk_][e_], ab + bo, kk_ * 8192 + e_ * 1024); }          \
    else { constexpr int m_ = (e_ - 2) / 6, f_ = ((e_ - 2) % 6) / 2, t_ = (e_ - 2) % 2;             \
      LDS_TR(wl[set][kk_][m_][f_][t_], ((f_ & 1) ? tb1 : tb0) + bo, kk_ * 6144 + m_ * 12288 + f_ * 32 + t_ * 768); } \
  } while (0)
#define RD4(st, set, r) do { RD1(st, set, r); RD1(st, set, r + 1); RD1(st, set, r + 2); RD1(st, set, r + 3); } while (0)
  WAIT_V(10);
  RAW_BAR();
  STAMP(1);
  RD4(0, 0, 0); RD4(0, 0, 4); RD4(0, 0, 8); RD4(0, 0, 12); RD4(0, 0, 16); RD4(0, 0, 20); RD4(0, 0, 24);
  const char* pd = (const char*)wd + (size_t)idx * 44032 + tid * 16;
  char* dump = sm + 3 * 40960 + wid * 1024;
#pragma unroll
  for (int s = 0; s < 8; ++s) {
    const int cur = s & 1, nx = cur ^ 1;
    if (s < 6) WAIT_V(5); else if (s == 6) WAIT_V(6);
    RAW_BAR();
    SCHED0();
    STAMP(2 + s);
    char* dst = sm + (s % 3) * 40960 + wid * 1024;
#define MM(kk, f, i) do {                                                                                   \
      ag[i][f] = __builtin_amdgcn_mfma_f32_16x16x32_bf16(CAT8(wl[cur][kk][0][f][0], wl[cur][kk][0][f][1]),    \
                                                         xb[cur][kk][i], ag[i][f], 0, 0, 0);                \
      au[i][f] = __builtin_amdgcn_mfma_f32_16x16x32_bf16(CAT8(wl[cur][kk][1][f][0], wl[cur][kk][1][f][1]),    \
                                                         xb[cur][kk][i], au[i][f], 0, 0, 0); } while (0)
#define DMA(j) do {                                                                                  \
      if (s + 3 < 8) {                                                                               \
        if (j == 0) GLDS(gA + (s + 3) * 16384, dst);                                                 \
        if (j == 1) GLDS(gA + (s + 3) * 16384 + 8192, dst + 8192);                                   \
        if (j >= 2 && j < 5) GLDS(gB[j >= 2 ? j - 2 : 0] + (size_t)(s + 3) * 64 * 2752, dst + 16384 + (j - 2) * 8192); \
      } else if (s == 5) {                                                                           \
        if (j < 5) GLDS(pd + j * 8192, dump);                                                        \
        else GLDS(pd + (tid < 192 ? 5 * 8192 : 0), dump);                                            \
      } } while (0)
#define RDN(r) do { if (s + 1 < 8) { RD1(s + 1, nx, r); RD1(s + 1, nx, r + 1); } } while (0)
#define RDN3(r) do { if (s + 1 < 8) { RD1(s + 1, nx, r); RD1(s + 1, nx, r + 1); RD1(s + 1, nx, r + 2); } } while (0)
    MM(0, 0, 0); SCHED0(); DMA(0); RDN(0);   SCHED0();
    MM(0, 0, 1); SCHED0(); RDN3(2);          SCHED0();
    MM(0, 1, 0); SCHED0(); DMA(1); RDN(5);   SCHED0();
    MM(0, 1, 1); SCHED0(); RDN3(7);          SCHED0();
    MM(0, 2, 0); SCHED0(); DMA(2); RDN(10);  SCHED0();
    MM(0, 2, 1); SCHED0(); RDN3(12);         SCHED0();
    MM(1, 0, 0); SCHED0(); DMA(3); RDN(15);  SCHED0();
    MM(1, 0, 1); SCHED0(); RDN3(17);         SCHED0();
    MM(1, 1, 0); SCHED0(); DMA(4); RDN(20);  SCHED0();
    MM(1, 1, 1); SCHED0(); RDN3(22);         SCHED0();
    MM(1, 2, 0); SCHED0(); DMA(5); RDN3(25); SCHED0();
    MM(1, 2, 1); SCHED0();
  }
  STAMP(10);
#pragma unroll
  for (int i = 0; i < 2; ++i) {
    int row = mt * 128 + wm * 32 + i * 16 + (lane & 15);
    int mt2 = row >> 6, rbl = (row >> 4) & 3;
#pragma unroll
    for (int f = 0; f < 3; ++f) {
      int c = wn * 48 + f * 16 + 4 * (lane >> 4);
      int k = 88 * ft + c;
      u2 o;
#pragma unroll
      for (int j = 0; j < 2; ++j) {
        unsigned gp = pack2(ag[i][f][2 * j], ag[i][f][2 * j + 1]), up = pack2(au[i][f][2 * j], au[i][f][2 * j + 1]);
        unsigned ep = pack2(gelu_t(bf2f(gp & 0xffffu)), gelu_t(bf2f_hi(gp)));
        o[j] = pack2(bf2f(ep & 0xffffu) * bf2f(up & 0xffffu), bf2f_hi(ep) * bf2f_hi(up));
      }
      size_t off = ((((size_t)mt2 * 43 + (k >> 5)) * 4 + rbl) * 64 + (lane & 15) + 16 * ((k & 31) >> 3)) * 8 + (k & 7);
      if (c < 88 && k < 1376) ST8((u2*)(act + off), o);
    }
  }
  WAIT_V(0);
  STAMP(11);
  STAMP_FLUSH(3);
}

__global__ __launch_bounds__(512) void k5_gemm2(const us* __restrict__ act, const us* __restrict__ wd,
                                                const us* __restrict__ h, us* __restrict__ out) {
  __shared__ __attribute__((aligned(16))) char sm[131072];
  const int tid = threadIdx.x, wid = tid >> 6, lane = tid & 63;
  const int xcd = blockIdx.x & 7, idx = blockIdx.x >> 3;
  const int mt2 = xcd * 4 + (idx >> 3), nt = idx & 7;
  STAMP_DECL; STAMP(0);
  char* ring = sm + wid * 16384;
  const char* gA = (const char*)act + ((size_t)mt2 * 43 * 4) * 1024 + lane * 16;
  const char* gW[4];
#pragma unroll
  for (int i = 0; i < 4; ++i) {
    int rho = 8 * i + (lane >> 3);
    int sw = ((rho >> 1) & 1) | (((rho >> 3) & 1) << 1);
    int ch = (lane & 7) ^ (sw << 1);
    gW[i] = (const char*)(wd + (size_t)rho * 512 + nt * 64 + ch * 8);
  }
  auto stage = [&](int kb, int buf) {
    char* dst = ring + buf * 8192;
#pragma unroll
    for (int i = 0; i < 4; ++i) GLDS(gA + (size_t)kb * 4096 + i * 1024, dst + i * 1024);
#pragma unroll
    for (int i = 0; i < 4; ++i) GLDS(gW[i] + (size_t)kb * 32 * 1024, dst + 4096 + i * 1024);
  };
  f4 acc[4][4];
#pragma unroll
  for (int i = 0; i < 4; ++i)
#pragma unroll
    for (int j = 0; j < 4; ++j) acc[i][j] = (f4){0, 0, 0, 0};
  const int g = lane >> 4, q4 = (lane & 15) >> 2, p4 = lane & 3;
  const int sw = ((q4 >> 1) & 1) | ((g & 1) << 1);
  const unsigned ringb = (unsigned)(size_t)sm + wid * 16384;
  const unsigned trb = ringb + 4096 + (8 * g + q4) * 128 + 8 * (p4 & 1) + 16 * (p4 >> 1);
  const unsigned tr0 = trb + 32 * (0 ^ sw), tr1 = trb + 32 * (1 ^ sw), tr2 = trb + 32 * (2 ^ sw), tr3 = trb + 32 * (3 ^ sw);
  const unsigned ab = ringb + lane * 16;
  const int nst = wid < 3 ? 6 : 5;
  stage(wid, 0);
  stage(wid + 8, 1);
#pragma unroll
  for (int i = 0; i < 6; ++i) {
    if (i < nst) {
      if (i + 1 < nst) WAIT_V(8); else WAIT_V(0);
      bf8 xa[4];
      s4v wl[4][2];
      LDS_B128(xa[0], ab, (i & 1) * 8192 + 0);
      LDS_B128(xa[1], ab, (i & 1) * 8192 + 1024);
      LDS_B128(xa[2], ab, (i & 1) * 8192 + 2048);
      LDS_B128(xa[3], ab, (i & 1) * 8192 + 3072);
      LDS_TR(wl[0][0], tr0, (i & 1) * 8192); LDS_TR(wl[0][1], tr0, (i & 1) * 8192 + 512);
      LDS_TR(wl[1][0], tr1, (i & 1) * 8192); LDS_TR(wl[1][1], tr1, (i & 1) * 8192 + 512);
      LDS_TR(wl[2][0], tr2, (i & 1) * 8192); LDS_TR(wl[2][1], tr2, (i & 1) * 8192 + 512);
      LDS_TR(wl[3][0], tr3, (i & 1) * 8192); LDS_TR(wl[3][1], tr3, (i & 1) * 8192 + 512);
      WAIT_L(0);
      SCHED0();
      if (i + 2 < nst) stage(wid + 8 * (i + 2), i & 1);
#pragma unroll
      for (int nb = 0; nb < 4; ++nb) {
        bf8 wb = CAT8(wl[nb][0], wl[nb][1]);
#pragma unroll
        for (int rb = 0; rb < 4; ++rb)
          acc[nb][rb] = __builtin_amdgcn_mfma_f32_16x16x32_bf16(wb, xa[rb], acc[nb][rb], 0, 0, 0);
      }
      SCHED0();
    }
  }
  STAMP(1);
  f4* red = (f4*)sm;
#pragma unroll
  for (int nb = 0; nb < 4; ++nb)
#pragma unroll
    for (int rb = 0; rb < 4; ++rb) red[(wid * 16 + nb * 4 + rb) * 64 + lane] = acc[nb][rb];
  __syncthreads();
  STAMP(2);
  const int rb = wid >> 1;
#pragma unroll
  for (int q = 0; q < 2; ++q) {
    int nb = 2 * (wid & 1) + q;
    f4 s = red[(nb * 4 + rb) * 64 + lane];
#pragma unroll
    for (int src = 1; src < 8; ++src) s += red[(src * 16 + nb * 4 + rb) * 64 + lane];
    int token = mt2 * 64 + rb * 16 + (lane & 15);
    int col = nt * 64 + nb * 16 + 4 * (lane >> 4);
    u2 hv = *(const u2*)(h + (size_t)token * 512 + col);
    float o0 = bf2f(hv[0] & 0xffffu) + rbf(s[0]);
    float o1 = bf2f_hi(hv[0]) + rbf(s[1]);
    float o2 = bf2f(hv[1] & 0xffffu) + rbf(s[2]);
    float o3 = bf2f_hi(hv[1]) + rbf(s[3]);
    *(u2*)(out + (size_t)token * 512 + col) = (u2){pack2(o0, o1), pack2(o2, o3)};
  }
  STAMP(3);
  STAMP_FLUSH(4);
}

extern "C" void kernel_launch(void* const* d_in, const int* in_sizes, int n_in, void* d_out, int out_size,
                              void* d_ws, size_t ws_size, hipStream_t stream) {
  const us* x = (const us*)d_in[0];
  const us* wr = (const us*)d_in[1];
  const us* wi = (const us*)d_in[2];
  const us* wg = (const us*)d_in[4];
  const us* wu = (const us*)d_in[5];
  const us* wd = (const us*)d_in[6];
  char* ws = (char*)d_ws;
  f2* X1 = (f2*)(ws);
  f2* X2 = (f2*)(ws + (5u << 20));
  us* h = (us*)(ws + (10u << 20));
  us* ln2 = (us*)(ws + (12u << 20));
  us* act = (us*)(ws + (14u << 20));
  unsigned* Wt = (unsigned*)(ws + (20u << 20));
  k1_lnfft<<<512, 256, 0, stream>>>(x, X1, wr, wi, Wt);
  k2_col<<<264 + 128, 256, 0, stream>>>(X1, X2, Wt, wg, wu);
  k3_ifft<<<256 + 128, 256, 0, stream>>>(X2, x, h, ln2, wg, wu);
  k4_gemm1<<<256, 512, 0, stream>>>(ln2, wg, wu, wd, act);
  k5_gemm2<<<256, 512, 0, stream>>>(act, wd, h, (us*)d_out);
}
```

```cpp
#include <hip/hip_runtime.h>
#include <hip/hip_bf16.h>
#include <stdint.h>

typedef float f2 __attribute__((ext_vector_type(2)));
typedef float f4 __attribute__((ext_vector_type(4)));
typedef unsigned u4 __attribute__((ext_vector_type(4)));
typedef unsigned u2 __attribute__((ext_vector_type(2)));
typedef short bf8 __attribute__((ext_vector_type(8)));
typedef unsigned short us;

#define WAVE_BAR() __builtin_amdgcn_wave_barrier()
#define ST16(ptr, val) asm volatile("global_store_dwordx4 %0, %1, off sc1\n\ts_nop 1" :: "v"(ptr), "v"(val) : "memory")
#define ST8(ptr, val) asm volatile("global_store_dwordx2 %0, %1, off sc1\n\ts_nop 1" :: "v"(ptr), "v"(val) : "memory")

__device__ __forceinline__ float bf2f(unsigned h) { return __uint_as_float(h << 16); }
__device__ __forceinline__ float bf2f_hi(unsigned h) { return __uint_as_float(h & 0xffff0000u); }
__device__ __forceinline__ unsigned f2bf(float f) {
  __bf16 b = (__bf16)f;
  return (unsigned)__builtin_bit_cast(unsigned short, b);
}
__device__ __forceinline__ float rbf(float f) { return bf2f(f2bf(f)); }
typedef __bf16 bf2v __attribute__((ext_vector_type(2)));
__device__ __forceinline__ unsigned pack2(float lo, float hi) {
  bf2v t = __builtin_convertvector((f2){lo, hi}, bf2v);
  return __builtin_bit_cast(unsigned, t);
}

__device__ __forceinline__ f2 cmul(f2 a, f2 b) { return (f2){a.x * b.x - a.y * b.y, a.x * b.y + a.y * b.x}; }
__device__ __forceinline__ f2 cmulc(f2 a, f2 b) { return (f2){a.x * b.x + a.y * b.y, a.y * b.x - a.x * b.y}; }
template <bool INV> __device__ __forceinline__ f2 rot(f2 a) { return INV ? (f2){-a.y, a.x} : (f2){a.y, -a.x}; }

template <bool INV> __device__ __forceinline__ void dft8(f2 (&v)[8]) {
  const float r = 0.70710678118654752f;
  f2 a0 = v[0] + v[4], a1 = v[0] - v[4], a2 = v[2] + v[6], a3 = rot<INV>(v[2] - v[6]);
  f2 a4 = v[1] + v[5], a5 = v[1] - v[5], a6 = v[3] + v[7], a7 = rot<INV>(v[3] - v[7]);
  f2 e0 = a0 + a2, e2 = a0 - a2, e1 = a1 + a3, e3 = a1 - a3;
  f2 o0 = a4 + a6, o2 = a4 - a6, o1 = a5 + a7, o3 = a5 - a7;
  o1 = (o1 + rot<INV>(o1)) * r;
  o2 = rot<INV>(o2);
  o3 = (rot<INV>(o3) - o3) * r;
  v[0] = e0 + o0; v[4] = e0 - o0;
  v[1] = e1 + o1; v[5] = e1 - o1;
  v[2] = e2 + o2; v[6] = e2 - o2;
  v[3] = e3 + o3; v[7] = e3 - o3;
}
template <bool INV> __device__ __forceinline__ void dft4(f2& x0, f2& x1, f2& x2, f2& x3) {
  f2 a0 = x0 + x2, a1 = x0 - x2, a2 = x1 + x3, a3 = rot<INV>(x1 - x3);
  x0 = a0 + a2; x2 = a0 - a2; x1 = a1 + a3; x3 = a1 - a3;
}
__device__ __forceinline__ void make_tw(f2 (&t)[8], int num, float c2) {
  float s, c;
  sincospif(-(float)num * c2, &s, &c);
  t[1] = (f2){c, s};
  t[2] = cmul(t[1], t[1]);
  t[3] = cmul(t[2], t[1]);
  t[4] = cmul(t[2], t[2]);
  t[5] = cmul(t[4], t[1]);
  t[6] = cmul(t[3], t[3]);
  t[7] = cmul(t[4], t[3]);
}

template <int ctrl, int rm> __device__ __forceinline__ float dpp_add(float v) {
  int t = __builtin_amdgcn_update_dpp(0, __float_as_int(v), ctrl, rm, 0xf, false);
  return v + __int_as_float(t);
}
__device__ __forceinline__ float wave_sum(float v) {
  v = dpp_add<0x111, 0xf>(v);
  v = dpp_add<0x112, 0xf>(v);
  v = dpp_add<0x114, 0xf>(v);
  v = dpp_add<0x118, 0xf>(v);
  v = dpp_add<0x142, 0xa>(v);
  v = dpp_add<0x143, 0xc>(v);
  return __int_as_float(__builtin_amdgcn_readlane(__float_as_int(v), 63));
}

__device__ __forceinline__ void ln_pair(float (&a)[8], float (&b)[8]) {
  float sa = 0.f, sb = 0.f;
#pragma unroll
  for (int j = 0; j < 8; ++j) { sa += a[j]; sb += b[j]; }
  sa = wave_sum(sa); sb = wave_sum(sb);
  float ma = sa * (1.0f / 512.0f), mb = sb * (1.0f / 512.0f);
  float va = 0.f, vb = 0.f;
#pragma unroll
  for (int j = 0; j < 8; ++j) { float d = a[j] - ma; va += d * d; float e = b[j] - mb; vb += e * e; }
  va = wave_sum(va) * (1.0f / 512.0f); vb = wave_sum(vb) * (1.0f / 512.0f);
  float mra = rbf(ma), mrb = rbf(mb);
  float da = rbf(sqrtf(rbf(rbf(va) + 1e-5f))), db = rbf(sqrtf(rbf(rbf(vb) + 1e-5f)));
  const float ra_ = 1.0f / da, rb_ = 1.0f / db;
#pragma unroll
  for (int j = 0; j < 8; ++j) {
    float na = rbf(a[j] - mra), nb = rbf(b[j] - mrb);
    float qa = na * ra_, qb = nb * rb_;
    qa = __builtin_fmaf(__builtin_fmaf(-qa, da, na), ra_, qa);
    qb = __builtin_fmaf(__builtin_fmaf(-qb, db, nb), rb_, qb);
    a[j] = rbf(qa); b[j] = rbf(qb);
  }
}

__device__ __forceinline__ int p_of_q(int q) {
  int T = q >> 3, e = q & 7;
  int k0 = T >> 5, k1 = (T >> 2) & 7, k2 = 2 * (T & 3) + (e >> 2), k3 = e & 3;
  return k0 + 8 * k1 + 64 * k2 + 512 * k3;
}

#define PFD 24
#define PF_DELAY() do { for (int i_ = 0; i_ < PFD; ++i_) __builtin_amdgcn_s_sleep(2); } while (0)
#define WSTR 584

__global__ __launch_bounds__(256) void k1_lnfft(const us* __restrict__ x, f2* __restrict__ X1,
                                                const us* __restrict__ wr, const us* __restrict__ wi,
                                                unsigned* __restrict__ Wt) {
  __shared__ __attribute__((aligned(16))) f2 lds[4 * WSTR];
  const int tid = threadIdx.x;
  if (blockIdx.x >= 256) {
    unsigned* tile = (unsigned*)lds;
    const int xg = blockIdx.x & 7, qi = (blockIdx.x - 256) >> 3;
    const int c = tid & 31;
    for (int i_ = 0; i_ < 8; ++i_) __builtin_amdgcn_s_sleep(2);
#pragma unroll
    for (int i = 0; i < 8; ++i) {
      int qq = (tid >> 5) + 8 * i;
      int pp = p_of_q(64 * qi + qq);
      unsigned r_ = wr[pp * 257 + 32 * xg + c], i_ = wi[pp * 257 + 32 * xg + c];
      tile[c * 65 + qq] = r_ | (i_ << 16);
    }
    if (xg == 0 && tid < 64) {
      int pp = p_of_q(64 * qi + tid);
      Wt[(size_t)256 * 2048 + 64 * qi + tid] = (unsigned)wr[pp * 257 + 256] | ((unsigned)wi[pp * 257 + 256] << 16);
    }
    __syncthreads();
#pragma unroll
    for (int i = 0; i < 2; ++i) {
      int cc = (tid >> 4) + 16 * i, q0 = 4 * (tid & 15);
      u4 o = (u4){tile[cc * 65 + q0], tile[cc * 65 + q0 + 1], tile[cc * 65 + q0 + 2], tile[cc * 65 + q0 + 3]};
      ST16((u4*)(Wt + (size_t)(32 * xg + cc) * 2048 + 64 * qi + q0), o);
    }
    return;
  }
  const int b = (blockIdx.x & 7) * 32 + (blockIdx.x >> 3);
  const int w = tid >> 6, l = tid & 63;

  const int row = b * 8 + w * 2;
  u4 ra = *(const u4*)(x + (size_t)row * 512 + l * 8);
  u4 rb = *(const u4*)(x + (size_t)(row + 1) * 512 + l * 8);
  f2 t1[8], t2[8];
  make_tw(t1, l, 2.0f / 512.0f);
  make_tw(t2, l & 7, 2.0f / 64.0f);

  float a[8], c[8];
#pragma unroll
  for (int j = 0; j < 4; ++j) {
    a[2 * j] = bf2f(ra[j] & 0xffffu); a[2 * j + 1] = bf2f_hi(ra[j]);
    c[2 * j] = bf2f(rb[j] & 0xffffu); c[2 * j + 1] = bf2f_hi(rb[j]);
  }
  ln_pair(a, c);

  f2* L = lds + w * WSTR;
  unsigned* lp = (unsigned*)L;
  u4 p0, p1;
#pragma unroll
  for (int j = 0; j < 4; ++j) {
    p0[j] = (__float_as_uint(a[j]) >> 16) | (__float_as_uint(c[j]) & 0xffff0000u);
    p1[j] = (__float_as_uint(a[j + 4]) >> 16) | (__float_as_uint(c[j + 4]) & 0xffff0000u);
  }
  *(u4*)(lp + l * 8) = p0;
  *(u4*)(lp + l * 8 + 4) = p1;
  WAVE_BAR();
  f2 v[8];
#pragma unroll
  for (int m = 0; m < 8; ++m) { unsigned q = lp[l + 64 * m]; v[m] = (f2){bf2f(q & 0xffffu), bf2f_hi(q)}; }
  WAVE_BAR();
  dft8<false>(v);
#pragma unroll
  for (int k = 1; k < 8; ++k) v[k] = cmul(v[k], t1[k]);
#pragma unroll
  for (int k = 0; k < 8; ++k) L[k * 72 + l] = v[k];
  WAVE_BAR();
  const int hi = l >> 3, lo = l & 7;
#pragma unroll
  for (int n = 0; n < 8; ++n) v[n] = L[hi * 72 + n * 8 + lo];
  WAVE_BAR();
  dft8<false>(v);
#pragma unroll
  for (int k = 1; k < 8; ++k) v[k] = cmul(v[k], t2[k]);
#pragma unroll
  for (int k = 0; k < 8; ++k) L[hi * 72 + k * 9 + lo] = v[k];
  WAVE_BAR();
#pragma unroll
  for (int n = 0; n < 8; ++n) v[n] = L[hi * 72 + lo * 9 + n];
  WAVE_BAR();
  dft8<false>(v);
#pragma unroll
  for (int k = 0; k < 8; ++k) L[hi + 9 * lo + 72 * k] = v[k];

  __syncthreads();

  const int p = tid & 3;
  const f2* Lp = lds + p * WSTR;
#pragma unroll
  for (int i = 0; i < 4; ++i) {
    int k = (tid >> 2) + 64 * i;
    int kk = (512 - k) & 511;
    f2 z = Lp[k + (k >> 3)];
    f2 y = Lp[kk + (kk >> 3)];
    f4 o = (f4){0.5f * (z.x + y.x), 0.5f * (z.y - y.y), 0.5f * (z.y + y.y), 0.5f * (y.x - z.x)};
    ST16((f4*)(X1 + (size_t)k * 2048 + b * 8 + 2 * p), o);
  }
  if (tid < 4) {
    f2 z = Lp[256 + 32];
    *(f4*)(X1 + (size_t)256 * 2048 + b * 8 + 2 * p) = (f4){z.x, 0.f, z.y, 0.f};
  }
}

__global__ __launch_bounds__(256) void k2_col(const f2* __restrict__ X1, f2* __restrict__ X2,
                                              const unsigned* __restrict__ Wt,
                                              const us* __restrict__ wg, const us* __restrict__ wu) {
  __shared__ __attribute__((aligned(16))) f2 lds[8 * 288];
  if (blockIdx.x >= 264) {
    const char* pg = (const char*)wg + ((blockIdx.x - 264) >> 3) * 88064 + threadIdx.x * 16;
    u4 acc = {0, 0, 0, 0};
    PF_DELAY();
#pragma unroll
    for (int i = 0; i < 21; ++i) acc ^= *(const u4*)(pg + i * 4096);
    if (threadIdx.x < 128) acc ^= *(const u4*)(pg + 21 * 4096);
    asm volatile("" ::"v"(acc));
    return;
  }
  if (blockIdx.x > 256) return;
  const int c = blockIdx.x < 256 ? (blockIdx.x & 7) * 32 + (blockIdx.x >> 3) : 256;
  const int t = threadIdx.x;
  const f2* col = X1 + (size_t)c * 2048;

  f2 v[8];
#pragma unroll
  for (int m = 0; m < 8; ++m) v[m] = col[t + 256 * m];
  u4 w0 = *(const u4*)(Wt + (size_t)c * 2048 + t * 8);
  u4 w1 = *(const u4*)(Wt + (size_t)c * 2048 + t * 8 + 4);
  f2 t1[8], t2[8], t3[8];
  make_tw(t1, t, 2.0f / 2048.0f);
  make_tw(t2, t & 31, 2.0f / 256.0f);
  make_tw(t3, t & 3, 2.0f / 32.0f);

  const int g = t >> 5, lo5 = t & 31;
  f2* R = lds + g * 288;
  dft8<false>(v);
#pragma unroll
  for (int k = 1; k < 8; ++k) v[k] = cmul(v[k], t1[k]);
#pragma unroll
  for (int k = 0; k < 8; ++k) lds[k * 288 + t] = v[k];

  __syncthreads();
#pragma unroll
  for (int n = 0; n < 8; ++n) v[n] = R[n * 32 + lo5];
  WAVE_BAR();
  dft8<false>(v);
#pragma unroll
  for (int k = 1; k < 8; ++k) v[k] = cmul(v[k], t2[k]);
#pragma unroll
  for (int k = 0; k < 8; ++k) R[k * 36 + lo5] = v[k];
  WAVE_BAR();
  const int m3 = (t >> 2) & 7, q3 = t & 3;
#pragma unroll
  for (int n = 0; n < 8; ++n) v[n] = R[m3 * 36 + 4 * n + q3];
  WAVE_BAR();
  dft8<false>(v);
#pragma unroll
  for (int k = 1; k < 8; ++k) v[k] = cmul(v[k], t3[k]);
#pragma unroll
  for (int k = 0; k < 8; ++k) R[m3 * 34 + 4 * k + q3] = v[k];
  WAVE_BAR();
#pragma unroll
  for (int e = 0; e < 8; ++e) v[e] = R[m3 * 34 + 8 * q3 + e];
  WAVE_BAR();
  dft4<false>(v[0], v[1], v[2], v[3]);
  dft4<false>(v[4], v[5], v[6], v[7]);

  {
    const float sc = 1.0f / 1048576.0f;
#pragma unroll
    for (int e = 0; e < 4; ++e) {
      v[e] = cmul(v[e], (f2){bf2f(w0[e] & 0xffffu) * sc, bf2f_hi(w0[e]) * sc});
      v[e + 4] = cmul(v[e + 4], (f2){bf2f(w1[e] & 0xffffu) * sc, bf2f_hi(w1[e]) * sc});
    }
  }

  dft4<true>(v[0], v[1], v[2], v[3]);
  dft4<true>(v[4], v[5], v[6], v[7]);
#pragma unroll
  for (int e = 0; e < 8; ++e) R[m3 * 34 + 8 * q3 + e] = v[e];
  WAVE_BAR();
#pragma unroll
  for (int k = 0; k < 8; ++k) v[k] = R[m3 * 34 + 4 * k + q3];
  WAVE_BAR();
#pragma unroll
  for (int k = 1; k < 8; ++k) v[k] = cmulc(v[k], t3[k]);
  dft8<true>(v);
#pragma unroll
  for (int n = 0; n < 8; ++n) R[m3 * 36 + 4 * n + q3] = v[n];
  WAVE_BAR();
#pragma unroll
  for (int k = 0; k < 8; ++k) v[k] = R[k * 36 + lo5];
  WAVE_BAR();
#pragma unroll
  for (int k = 1; k < 8; ++k) v[k] = cmulc(v[k], t2[k]);
  dft8<true>(v);
#pragma unroll
  for (int n = 0; n < 8; ++n) R[n * 32 + lo5] = v[n];
  __syncthreads();
#pragma unroll
  for (int k = 0; k < 8; ++k) v[k] = lds[k * 288 + t];
#pragma unroll
  for (int k = 1; k < 8; ++k) v[k] = cmulc(v[k], t1[k]);
  dft8<true>(v);

  f2* ocol = X2 + (size_t)c * 2048;
#pragma unroll
  for (int m = 0; m < 8; ++m) ST8(ocol + t + 256 * m, v[m]);
}

__global__ __launch_bounds__(256) void k3_ifft(const f2* __restrict__ X2, const us* __restrict__ x,
                                               us* __restrict__ h, us* __restrict__ ln2,
                                               const us* __restrict__ wg, const us* __restrict__ wu) {
  __shared__ __attribute__((aligned(16))) f2 lds[4 * WSTR];
  const int tid = threadIdx.x;
  if (blockIdx.x >= 256) {
    const char* pu = (const char*)wu + ((blockIdx.x - 256) >> 3) * 88064 + threadIdx.x * 16;
    u4 acc = {0, 0, 0, 0};
    PF_DELAY();
#pragma unroll
    for (int i = 0; i < 21; ++i) acc ^= *(const u4*)(pu + i * 4096);
    if (threadIdx.x < 128) acc ^= *(const u4*)(pu + 21 * 4096);
    asm volatile("" ::"v"(acc));
    return;
  }
  const int b = (blockIdx.x & 7) * 32 + (blockIdx.x >> 3);
  const int w = tid >> 6, l = tid & 63;
  const int p = tid & 3;

  f4 o[4];
#pragma unroll
  for (int i = 0; i < 4; ++i) {
    int k = (tid >> 2) + 64 * i;
    o[i] = *(const f4*)(X2 + (size_t)k * 2048 + b * 8 + 2 * p);
  }
  f4 ony = *(const f4*)(X2 + (size_t)256 * 2048 + b * 8 + 2 * p);
  const int row = b * 8 + w * 2;
  u4 ra = *(const u4*)(x + (size_t)row * 512 + l * 8);
  u4 rb = *(const u4*)(x + (size_t)(row + 1) * 512 + l * 8);
  f2 t1[8], t2[8];
  make_tw(t1, l, 2.0f / 512.0f);
  make_tw(t2, l & 7, 2.0f / 64.0f);

  f2* Lp = lds + p * WSTR;
#pragma unroll
  for (int i = 0; i < 4; ++i) {
    int k = (tid >> 2) + 64 * i;
    int kk = (512 - k) & 511;
    f4 z = o[i];
    if (k == 0) {
      Lp[0] = (f2){z.x, z.z};
      Lp[256 + 32] = (f2){ony.x, ony.z};
    } else {
      Lp[k + (k >> 3)] = (f2){z.x - z.w, z.y + z.z};
      Lp[kk + (kk >> 3)] = (f2){z.x + z.w, z.z - z.y};
    }
  }
  __syncthreads();

  f2* L = lds + w * WSTR;
  const int hi = l >> 3, lo = l & 7;
  f2 v[8];
#pragma unroll
  for (int k = 0; k < 8; ++k) v[k] = L[hi + 9 * lo + 72 * k];
  WAVE_BAR();
  dft8<true>(v);
#pragma unroll
  for (int n = 1; n < 8; ++n) v[n] = cmulc(v[n], t2[n]);
#pragma unroll
  for (int n = 0; n < 8; ++n) L[hi * 72 + lo * 9 + n] = v[n];
  WAVE_BAR();
#pragma unroll
  for (int k = 0; k < 8; ++k) v[k] = L[hi * 72 + k * 9 + lo];
  WAVE_BAR();
  dft8<true>(v);
#pragma unroll
  for (int n = 0; n < 8; ++n) L[hi * 72 + n * 8 + lo] = v[n];
  WAVE_BAR();
#pragma unroll
  for (int k = 0; k < 8; ++k) v[k] = L[k * 72 + l];
  WAVE_BAR();
#pragma unroll
  for (int k = 1; k < 8; ++k) v[k] = cmulc(v[k], t1[k]);
  dft8<true>(v);

  unsigned* lp = (unsigned*)L;
#pragma unroll
  for (int m = 0; m < 8; ++m) lp[l + 64 * m] = pack2(v[m].x, v[m].y);
  WAVE_BAR();
  u4 y0 = *(const u4*)(lp + l * 8), y1 = *(const u4*)(lp + l * 8 + 4);
  float a[8], c[8];
#pragma unroll
  for (int j = 0; j < 4; ++j) {
    unsigned ya = y0[j], yb = y1[j];
    float xa0 = (j & 1) ? bf2f_hi(ra[j >> 1]) : bf2f(ra[j >> 1] & 0xffffu);
    float xb0 = (j & 1) ? bf2f_hi(rb[j >> 1]) : bf2f(rb[j >> 1] & 0xffffu);
    float xa1 = (j & 1) ? bf2f_hi(ra[2 + (j >> 1)]) : bf2f(ra[2 + (j >> 1)] & 0xffffu);
    float xb1 = (j & 1) ? bf2f_hi(rb[2 + (j >> 1)]) : bf2f(rb[2 + (j >> 1)] & 0xffffu);
    a[j] = rbf(xa0 + bf2f(ya & 0xffffu));
    c[j] = rbf(xb0 + bf2f_hi(ya));
    a[j + 4] = rbf(xa1 + bf2f(yb & 0xffffu));
    c[j + 4] = rbf(xb1 + bf2f_hi(yb));
  }
  u4 ha, hb;
#pragma unroll
  for (int j = 0; j < 4; ++j) {
    ha[j] = (__float_as_uint(a[2 * j]) >> 16) | (__float_as_uint(a[2 * j + 1]) & 0xffff0000u);
    hb[j] = (__float_as_uint(c[2 * j]) >> 16) | (__float_as_uint(c[2 * j + 1]) & 0xffff0000u);
  }
  ST16((u4*)(h + (size_t)row * 512 + l * 8), ha);
  ST16((u4*)(h + (size_t)(row + 1) * 512 + l * 8), hb);
  ln_pair(a, c);
#pragma unroll
  for (int j = 0; j < 4; ++j) {
    ha[j] = (__float_as_uint(a[2 * j]) >> 16) | (__float_as_uint(a[2 * j + 1]) & 0xffff0000u);
    hb[j] = (__float_as_uint(c[2 * j]) >> 16) | (__float_as_uint(c[2 * j + 1]) & 0xffff0000u);
  }
  {
    int mt = row >> 7, rbk = (row >> 4) & 7, kb = l >> 2, ch = l & 3;
    size_t base = (((size_t)mt * 16 + kb) * 8 + rbk) * 64 + 16 * ch;
    *(u4*)(ln2 + (base + (row & 15)) * 8) = ha;
    *(u4*)(ln2 + (base + ((row + 1) & 15)) * 8) = hb;
  }
}

#define GLDS(gp, lp) __builtin_amdgcn_global_load_lds((const unsigned*)(gp), (unsigned*)(lp), 16, 0, 0)
#define WAIT_V(n) asm volatile("s_waitcnt vmcnt(" #n ")" ::: "memory")
#define WAIT_L0() asm volatile("s_waitcnt lgkmcnt(0)" ::: "memory")
#define RAW_BAR() do { asm volatile("s_waitcnt lgkmcnt(0)" ::: "memory"); __builtin_amdgcn_s_barrier(); } while (0)
typedef short s4v __attribute__((ext_vector_type(4)));
#define LDS_TR(dst, addr, off) asm volatile("ds_read_b64_tr_b16 %0, %1 offset:%2" : "=v"(dst) : "v"(addr), "i"(off))
#define LDS_B128(dst, addr, off) asm volatile("ds_read_b128 %0, %1 offset:%2" : "=v"(dst) : "v"(addr), "i"(off))
#define WAIT_L(n) asm volatile("s_waitcnt lgkmcnt(" #n ")" ::: "memory")
#define CAT8(a, b) ((bf8){a[0], a[1], a[2], a[3], b[0], b[1], b[2], b[3]})
#define SCHED0() __builtin_amdgcn_sched_barrier(0)

__device__ __forceinline__ float gelu_t(float x) {
  float y2 = -2.3022082007793f * (x + 0.044715f * x * x * x);
  return x * __builtin_amdgcn_rcpf(1.0f + __builtin_amdgcn_exp2f(y2));
}

__global__ __launch_bounds__(512) void k4_gemm1(const us* __restrict__ ln2, const us* __restrict__ wg,
                                                const us* __restrict__ wu, const us* __restrict__ wd,
                                                us* __restrict__ act) {
  __shared__ __attribute__((aligned(16))) char sm[3 * 40960 + 8192];
  const int tid = threadIdx.x, wid = tid >> 6, lane = tid & 63;
  const int xcd = blockIdx.x & 7, idx = blockIdx.x >> 3;
  const int mt = 2 * xcd + (idx >> 4), ft = idx & 15;
  const int wm = wid >> 1, wn = wid & 1;

  const char* gA = (const char*)ln2 + (size_t)mt * 131072 + tid * 16;
  const char* gB[3];
#pragma unroll
  for (int i = 0; i < 3; ++i) {
    int q = i * 512 + tid;
    int mat = q >= 768 ? 1 : 0;
    int qq = q - mat * 768;
    int r = qq / 12, cp = qq - r * 12;
    int cch = cp ^ (((r >> 3) & 1) << 1);
    int col0 = 88 * ft + 8 * cch;
    col0 = col0 > 1368 ? 1368 : col0;
    gB[i] = (const char*)((mat ? wu : wg) + (size_t)r * 1376 + col0);
  }
  auto stage = [&](int s, int buf) {
    char* dst = sm + buf * 40960 + wid * 1024;
    GLDS(gA + s * 16384, dst);
    GLDS(gA + s * 16384 + 8192, dst + 8192);
    GLDS(gB[0] + (size_t)s * 64 * 2752, dst + 16384);
    GLDS(gB[1] + (size_t)s * 64 * 2752, dst + 16384 + 8192);
    GLDS(gB[2] + (size_t)s * 64 * 2752, dst + 16384 + 16384);
  };
  f4 ag[2][3], au[2][3];
#pragma unroll
  for (int i = 0; i < 2; ++i)
#pragma unroll
    for (int f = 0; f < 3; ++f) { ag[i][f] = (f4){0, 0, 0, 0}; au[i][f] = (f4){0, 0, 0, 0}; }

  stage(0, 0);

  __builtin_amdgcn_s_barrier();
  stage(1, 1);
  stage(2, 2);

  const int g = lane >> 4, q4 = (lane & 15) >> 2, p4 = lane & 3;
  const unsigned smb = (unsigned)(size_t)sm;
  const unsigned tb = smb + 16384 + (8 * g + q4) * 192 + 8 * (p4 & 1) + 16 * (p4 >> 1) + 96 * wn;
  const int sgn = 32 * (g & 1);
  const unsigned tb0 = tb + (wn ? -sgn : sgn), tb1 = tb + (wn ? sgn : -sgn);
  const unsigned ab = smb + lane * 16 + wm * 2048;
  bf8 xb[2][2][2];
  s4v wl[2][2][2][3][2];
#define RD1(st, set, r) do {                                                                        \
    const unsigned bo = ((st) % 3) * 40960;                                                         \
    constexpr int kk_ = (r) / 14, e_ = (r) % 14;                                                    \
    if constexpr (e_ < 2) { LDS_B128(xb[set][kk_][e_], ab + bo, kk_ * 8192 + e_ * 1024); }          \
    else { constexpr int m_ = (e_ - 2) / 6, f_ = ((e_ - 2) % 6) / 2, t_ = (e_ - 2) % 2;             \
      LDS_TR(wl[set][kk_][m_][f_][t_], ((f_ & 1) ? tb1 : tb0) + bo, kk_ * 6144 + m_ * 12288 + f_ * 32 + t_ * 768); } \
  } while (0)
#define RD4(st, set, r) do { RD1(st, set, r); RD1(st, set, r + 1); RD1(st, set, r + 2); RD1(st, set, r + 3); } while (0)
  WAIT_V(10);
  RAW_BAR();

  RD4(0, 0, 0); RD4(0, 0, 4); RD4(0, 0, 8); RD4(0, 0, 12); RD4(0, 0, 16); RD4(0, 0, 20); RD4(0, 0, 24);
  const char* pd = (const char*)wd + (size_t)idx * 44032 + tid * 16;
  char* dump = sm + 3 * 40960 + wid * 1024;
#pragma unroll
  for (int s = 0; s < 8; ++s) {
    const int cur = s & 1, nx = cur ^ 1;
    if (s < 6) WAIT_V(5); else if (s == 6) WAIT_V(6);
    RAW_BAR();
    SCHED0();

    char* dst = sm + (s % 3) * 40960 + wid * 1024;
#define MM(kk, f, i) do {                                                                                   \
      ag[i][f] = __builtin_amdgcn_mfma_f32_16x16x32_bf16(CAT8(wl[cur][kk][0][f][0], wl[cur][kk][0][f][1]),    \
                                                         xb[cur][kk][i], ag[i][f], 0, 0, 0);                \
      au[i][f] = __builtin_amdgcn_mfma_f32_16x16x32_bf16(CAT8(wl[cur][kk][1][f][0], wl[cur][kk][1][f][1]),    \
                                                         xb[cur][kk][i], au[i][f], 0, 0, 0); } while (0)
#define DMA(j) do {                                                                                  \
      if (s + 3 < 8) {                                                                               \
        if (j == 0) GLDS(gA + (s + 3) * 16384, dst);                                                 \
        if (j == 1) GLDS(gA + (s + 3) * 16384 + 8192, dst + 8192);                                   \
        if (j >= 2 && j < 5) GLDS(gB[j >= 2 ? j - 2 : 0] + (size_t)(s + 3) * 64 * 2752, dst + 16384 + (j - 2) * 8192); \
      } else if (s == 5) {                                                                           \
        if (j < 5) GLDS(pd + j * 8192, dump);                                                        \
        else GLDS(pd + (tid < 192 ? 5 * 8192 : 0), dump);                                            \
      } } while (0)
#define RDN(r) do { if (s + 1 < 8) { RD1(s + 1, nx, r); RD1(s + 1, nx, r + 1); } } while (0)
#define RDN3(r) do { if (s + 1 < 8) { RD1(s + 1, nx, r); RD1(s + 1, nx, r + 1); RD1(s + 1, nx, r + 2); } } while (0)
    MM(0, 0, 0); SCHED0(); DMA(0); RDN(0);   SCHED0();
    MM(0, 0, 1); SCHED0(); RDN3(2);          SCHED0();
    MM(0, 1, 0); SCHED0(); DMA(1); RDN(5);   SCHED0();
    MM(0, 1, 1); SCHED0(); RDN3(7);          SCHED0();
    MM(0, 2, 0); SCHED0(); DMA(2); RDN(10);  SCHED0();
    MM(0, 2, 1); SCHED0(); RDN3(12);         SCHED0();
    MM(1, 0, 0); SCHED0(); DMA(3); RDN(15);  SCHED0();
    MM(1, 0, 1); SCHED0(); RDN3(17);         SCHED0();
    MM(1, 1, 0); SCHED0(); DMA(4); RDN(20);  SCHED0();
    MM(1, 1, 1); SCHED0(); RDN3(22);         SCHED0();
    MM(1, 2, 0); SCHED0(); DMA(5); RDN3(25); SCHED0();
    MM(1, 2, 1); SCHED0();
  }

#pragma unroll
  for (int i = 0; i < 2; ++i) {
    int row = mt * 128 + wm * 32 + i * 16 + (lane & 15);
    int mt2 = row >> 6, rbl = (row >> 4) & 3;
#pragma unroll
    for (int f = 0; f < 3; ++f) {
      int c = wn * 48 + f * 16 + 4 * (lane >> 4);
      int k = 88 * ft + c;
      u2 o;
#pragma unroll
      for (int j = 0; j < 2; ++j) {
        unsigned gp = pack2(ag[i][f][2 * j], ag[i][f][2 * j + 1]), up = pack2(au[i][f][2 * j], au[i][f][2 * j + 1]);
        unsigned ep = pack2(gelu_t(bf2f(gp & 0xffffu)), gelu_t(bf2f_hi(gp)));
        o[j] = pack2(bf2f(ep & 0xffffu) * bf2f(up & 0xffffu), bf2f_hi(ep) * bf2f_hi(up));
      }
      size_t off = ((((size_t)mt2 * 43 + (k >> 5)) * 4 + rbl) * 64 + (lane & 15) + 16 * ((k & 31) >> 3)) * 8 + (k & 7);
      if (c < 88 && k < 1376) ST8((u2*)(act + off), o);
    }
  }
  WAIT_V(0);
}

__global__ __launch_bounds__(512) void k5_gemm2(const us* __restrict__ act, const us* __restrict__ wd,
                                                const us* __restrict__ h, us* __restrict__ out) {
  __shared__ __attribute__((aligned(16))) char sm[131072];
  const int tid = threadIdx.x, wid = tid >> 6, lane = tid & 63;
  const int xcd = blockIdx.x & 7, idx = blockIdx.x >> 3;
  const int mt2 = xcd * 4 + (idx >> 3), nt = idx & 7;

  char* ring = sm + wid * 16384;
  const char* gA = (const char*)act + ((size_t)mt2 * 43 * 4) * 1024 + lane * 16;
  const char* gW[4];
#pragma unroll
  for (int i = 0; i < 4; ++i) {
    int rho = 8 * i + (lane >> 3);
    int sw = ((rho >> 1) & 1) | (((rho >> 3) & 1) << 1);
    int ch = (lane & 7) ^ (sw << 1);
    gW[i] = (const char*)(wd + (size_t)rho * 512 + nt * 64 + ch * 8);
  }
  auto stage = [&](int kb, int buf) {
    char* dst = ring + buf * 8192;
#pragma unroll
    for (int i = 0; i < 4; ++i) GLDS(gA + (size_t)kb * 4096 + i * 1024, dst + i * 1024);
#pragma unroll
    for (int i = 0; i < 4; ++i) GLDS(gW[i] + (size_t)kb * 32 * 1024, dst + 4096 + i * 1024);
  };
  f4 acc[4][4];
#pragma unroll
  for (int i = 0; i < 4; ++i)
#pragma unroll
    for (int j = 0; j < 4; ++j) acc[i][j] = (f4){0, 0, 0, 0};
  const int g = lane >> 4, q4 = (lane & 15) >> 2, p4 = lane & 3;
  const int sw = ((q4 >> 1) & 1) | ((g & 1) << 1);
  const unsigned ringb = (unsigned)(size_t)sm + wid * 16384;
  const unsigned trb = ringb + 4096 + (8 * g + q4) * 128 + 8 * (p4 & 1) + 16 * (p4 >> 1);
  const unsigned tr0 = trb + 32 * (0 ^ sw), tr1 = trb + 32 * (1 ^ sw), tr2 = trb + 32 * (2 ^ sw), tr3 = trb + 32 * (3 ^ sw);
  const unsigned ab = ringb + lane * 16;
  const int nst = wid < 3 ? 6 : 5;
  stage(wid, 0);
  stage(wid + 8, 1);
#pragma unroll
  for (int i = 0; i < 6; ++i) {
    if (i < nst) {
      if (i + 1 < nst) WAIT_V(8); else WAIT_V(0);
      bf8 xa[4];
      s4v wl[4][2];
      LDS_B128(xa[0], ab, (i & 1) * 8192 + 0);
      LDS_B128(xa[1], ab, (i & 1) * 8192 + 1024);
      LDS_B128(xa[2], ab, (i & 1) * 8192 + 2048);
      LDS_B128(xa[3], ab, (i & 1) * 8192 + 3072);
      LDS_TR(wl[0][0], tr0, (i & 1) * 8192); LDS_TR(wl[0][1], tr0, (i & 1) * 8192 + 512);
      LDS_TR(wl[1][0], tr1, (i & 1) * 8192); LDS_TR(wl[1][1], tr1, (i & 1) * 8192 + 512);
      LDS_TR(wl[2][0], tr2, (i & 1) * 8192); LDS_TR(wl[2][1], tr2, (i & 1) * 8192 + 512);
      LDS_TR(wl[3][0], tr3, (i & 1) * 8192); LDS_TR(wl[3][1], tr3, (i & 1) * 8192 + 512);
      WAIT_L(0);
      SCHED0();
      if (i + 2 < nst) stage(wid + 8 * (i + 2), i & 1);
#pragma unroll
      for (int nb = 0; nb < 4; ++nb) {
        bf8 wb = CAT8(wl[nb][0], wl[nb][1]);
#pragma unroll
        for (int rb = 0; rb < 4; ++rb)
          acc[nb][rb] = __builtin_amdgcn_mfma_f32_16x16x32_bf16(wb, xa[rb], acc[nb][rb], 0, 0, 0);
      }
      SCHED0();
    }
  }

  f4* red = (f4*)sm;
#pragma unroll
  for (int nb = 0; nb < 4; ++nb)
#pragma unroll
    for (int rb = 0; rb < 4; ++rb) red[(wid * 16 + nb * 4 + rb) * 64 + lane] = acc[nb][rb];
  __syncthreads();

  const int rb = wid >> 1;
#pragma unroll
  for (int q = 0; q < 2; ++q) {
    int nb = 2 * (wid & 1) + q;
    f4 s = red[(nb * 4 + rb) * 64 + lane];
#pragma unroll
    for (int src = 1; src < 8; ++src) s += red[(src * 16 + nb * 4 + rb) * 64 + lane];
    int token = mt2 * 64 + rb * 16 + (lane & 15);
    int col = nt * 64 + nb * 16 + 4 * (lane >> 4);
    u2 hv = *(const u2*)(h + (size_t)token * 512 + col);
    float o0 = bf2f(hv[0] & 0xffffu) + rbf(s[0]);
    float o1 = bf2f_hi(hv[0]) + rbf(s[1]);
    float o2 = bf2f(hv[1] & 0xffffu) + rbf(s[2]);
    float o3 = bf2f_hi(hv[1]) + rbf(s[3]);
    *(u2*)(out + (size_t)token * 512 + col) = (u2){pack2(o0, o1), pack2(o2, o3)};
  }
}

extern "C" void kernel_launch(void* const* d_in, const int* in_sizes, int n_in, void* d_out, int out_size,
                              void* d_ws, size_t ws_size, hipStream_t stream) {
  const us* x = (const us*)d_in[0];
  const us* wr = (const us*)d_in[1];
  const us* wi = (const us*)d_in[2];
  const us* wg = (const us*)d_in[4];
  const us* wu = (const us*)d_in[5];
  const us* wd = (const us*)d_in[6];
  char* ws = (char*)d_ws;
  f2* X1 = (f2*)(ws);
  f2* X2 = (f2*)(ws + (5u << 20));
  us* h = (us*)(ws + (10u << 20));
  us* ln2 = (us*)(ws + (12u << 20));
  us* act = (us*)(ws + (14u << 20));
  unsigned* Wt = (unsigned*)(ws + (20u << 20));
  k1_lnfft<<<512, 256, 0, stream>>>(x, X1, wr, wi, Wt);
  k2_col<<<264 + 128, 256, 0, stream>>>(X1, X2, Wt, wg, wu);
  k3_ifft<<<256 + 128, 256, 0, stream>>>(X2, x, h, ln2, wg, wu);
  k4_gemm1<<<256, 512, 0, stream>>>(ln2, wg, wu, wd, act);
  k5_gemm2<<<256, 512, 0, stream>>>(act, wd, h, (us*)d_out);
}
```
